```python
import torch

def _softmax(x, axis=-1):
    return torch.softmax(x, dim=axis)
def _gelu(x):
    return torch.nn.functional.gelu(x, approximate="tanh")
def _silu(x):
    return torch.nn.functional.silu(x)
def _sigmoid(x):
    return torch.sigmoid(x)
def _layernorm(x, axis=-1, eps=1e-5):
    m = x.mean(dim=axis, keepdim=True)
    v = x.var(dim=axis, keepdim=True, unbiased=False)
    return (x - m) / torch.sqrt(v + eps)
def _scatter_add(tgt, idx, src, axis=0):
    return tgt.clone().index_add_(axis, idx, src)
def _segment_sum(src, seg, n):
    out = torch.zeros((n,) + src.shape[1:], dtype=src.dtype)
    return out.index_add_(0, seg, src)
def _pad(x, pw):
    flat = tuple(v for pair in reversed(pw) for v in pair)
    return torch.nn.functional.pad(x, flat)

kernel_name = 'sequential_preLN__sliding_block_cached__dense_activation_silu__decode'

def setup_inputs():

    _w = (lambda a: (lambda t: t.to(torch.bfloat16) if t.is_floating_point() else t)(torch.as_tensor(a)))
    def _A_setup():
        B, Sq, D = 1, 1, 576
        Hq, Hkv, Sk, Dh = 32, 16, 8192, 128
        assert Hq % Hkv == 0, (Hq, Hkv)
        return {
            "x": _w((lambda *s: torch.randn(*s, dtype=torch.bfloat16) * 0.05)(B, Sq, D)),
            "k_cache": _w((lambda *s: torch.randn(*s, dtype=torch.bfloat16) * 3.2)(B, Hkv, Sk, Dh)),
            "v_cache": _w((lambda *s: torch.randn(*s, dtype=torch.bfloat16) * 0.8)(B, Hkv, Sk, Dh)),
            "wq": _w((lambda *s: torch.randn(*s, dtype=torch.bfloat16) * 3.2)(D, Hq * Dh) * (D ** -0.5)),
            "wo": _w((lambda *s: torch.randn(*s, dtype=torch.bfloat16) * 0.8)(Hq * Dh, D) * ((Hq * Dh) ** -0.5)),
        }
    _A_in = _A_setup()

    def _B_setup():
        B, S, D, F = 1, 1, 576, 4608
        return {
            "x": _w((lambda *s: torch.randn(*s, dtype=torch.bfloat16) * 0.05)(B, S, D)),
            "w1": _w((lambda *s: torch.randn(*s, dtype=torch.bfloat16) * 3.2)(D, F) * (D ** -0.5)),
            "w2": _w((lambda *s: torch.randn(*s, dtype=torch.bfloat16) * 0.4)(F, D) * (F ** -0.5)),
        }
    _B_in = _B_setup()

    return {
        "A_x": _A_in["x"],
        "A_k_cache": _A_in["k_cache"],
        "A_v_cache": _A_in["v_cache"],
        "A_wq": _A_in["wq"],
        "A_wo": _A_in["wo"],
        "B_x": _B_in["x"],
        "B_w1": _B_in["w1"],
        "B_w2": _B_in["w2"],
    }

def reference(A_x, A_k_cache, A_v_cache, A_wq, A_wo, B_x, B_w1, B_w2):
    def _A_ref(x, k_cache, v_cache, wq, wo):
        B, Sq, D = x.shape
        Hq, Dh = 32, 128
        Hkv, Sk = k_cache.shape[1], k_cache.shape[2]
        W = min(4096, max(256, Sk // 4))
        rep = Hq // Hkv
        q = (lambda x, *d: x.permute(*d))((x @ wq).reshape(B, Sq, Hq, Dh), 0, 2, 1, 3)
        k = (lambda x, n, axis: torch.repeat_interleave(x, n, dim=axis))(k_cache[:, :, Sk - W:, :], rep, axis=1)
        v = (lambda x, n, axis: torch.repeat_interleave(x, n, dim=axis))(v_cache[:, :, Sk - W:, :], rep, axis=1)
        scores = torch.einsum("bhqd,bhkd->bhqk", q, k) * (Dh ** -0.5)
        p = _softmax(scores, axis=-1)
        o = torch.einsum("bhqk,bhkd->bhqd", p, v)
        return (lambda x, *d: x.permute(*d))(o, 0, 2, 1, 3).reshape(B, Sq, Hq * Dh) @ wo

    def _B_ref(x, w1, w2):
        h = torch.einsum("bsd,df->bsf", x, w1)
        h = _silu(h)
        out = torch.einsum("bsf,fd->bsd", h, w2)
        return out

    x = A_x
    h = x + _A_ref(_layernorm(x, axis=-1), A_k_cache, A_v_cache, A_wq, A_wo)
    h2 = h + _B_ref(_layernorm(h, axis=-1), B_w1, B_w2)
    return h2
```

```cpp
#include <hip/hip_runtime.h>
#include <hip/hip_bf16.h>
#include <stdint.h>

#define MIMIC 1

typedef unsigned short u16;
constexpr int D = 576, HQ = 32, HKV = 16, SK = 8192, DH = 128, W = 2048, F = 4608;
constexpr int NSPLIT = 16, KPS = W / NSPLIT;

__device__ __forceinline__ float bf2f(u16 b) { return __uint_as_float(((unsigned)b) << 16); }
__device__ __forceinline__ u16 f2bf(float f) { unsigned u = __float_as_uint(f); u += 0x7FFFu + ((u >> 16) & 1u); return (u16)(u >> 16); }
__device__ __forceinline__ float rbf(float f) {
  return bf2f(f2bf(f));
}

__device__ void layernorm_block(const float* xs, float* xn, float* red) {
  int tid = threadIdx.x;
  float s = 0.f;
  for (int i = tid; i < D; i += 256) s += xs[i];
  for (int o = 32; o; o >>= 1) s += __shfl_xor(s, o);
  if ((tid & 63) == 0) red[tid >> 6] = s;
  __syncthreads();
  float mean = (red[0] + red[1] + red[2] + red[3]) / D;
  __syncthreads();
  float v = 0.f;
  for (int i = tid; i < D; i += 256) { float d = xs[i] - mean; v += d * d; }
  for (int o = 32; o; o >>= 1) v += __shfl_xor(v, o);
  if ((tid & 63) == 0) red[tid >> 6] = v;
  __syncthreads();
  float var = (red[0] + red[1] + red[2] + red[3]) / D;
  __syncthreads();
  float mb = rbf(mean);
  float sd = rbf(sqrtf(rbf(rbf(var) + 1e-5f)));
  for (int i = tid; i < D; i += 256) xn[i] = rbf(rbf(xs[i] - mb) / sd);
  __syncthreads();
}

__global__ void __launch_bounds__(256) k_q(const u16* x, const u16* wq, float* q) {
  __shared__ float xs[D], xn[D], red[4], part[256];
  int tid = threadIdx.x;
  for (int i = tid; i < D; i += 256) xs[i] = bf2f(x[i]);
  __syncthreads();
  layernorm_block(xs, xn, red);
  int col = blockIdx.x * 64 + (tid & 63), kq = tid >> 6;
  float acc = 0.f;
  for (int k = kq * 144; k < kq * 144 + 144; ++k) acc += xn[k] * bf2f(wq[(size_t)k * (HQ * DH) + col]);
  part[tid] = acc;
  __syncthreads();
  if (tid < 64) q[col] = rbf(part[tid] + part[tid + 64] + part[tid + 128] + part[tid + 192]);
}

__global__ void __launch_bounds__(256) k_attn(const float* q, const u16* kc, const u16* vc, float* po, float* pml) {
  __shared__ float qs[2 * DH], sc[2][KPS], red[8];
  int g = blockIdx.x / NSPLIT, s = blockIdx.x % NSPLIT, tid = threadIdx.x;
  qs[tid] = q[g * 256 + tid];
  __syncthreads();
  const u16* kb = kc + ((size_t)g * SK + (SK - W) + s * KPS) * DH;
  const u16* vb = vc + ((size_t)g * SK + (SK - W) + s * KPS) * DH;
  {
    int h = tid >> 7, key = tid & 127;
    float acc = 0.f;
    for (int d = 0; d < DH; ++d) acc += qs[h * DH + d] * bf2f(kb[key * DH + d]);
    sc[h][key] = rbf(rbf(acc) * 0.08838834764831845f);
  }
  __syncthreads();
  {
    int h = tid >> 7, d = tid & 127;
    float m = -INFINITY;
    for (int k = 0; k < KPS; ++k) m = fmaxf(m, sc[h][k]);
    float l = 0.f, o = 0.f;
    for (int k = 0; k < KPS; ++k) { float p = __expf(sc[h][k] - m); l += p; o += p * bf2f(vb[k * DH + d]); }
    int hq = g * 2 + h;
    po[((size_t)hq * NSPLIT + s) * DH + d] = o;
    if (d == 0) { pml[(hq * NSPLIT + s) * 2] = m; pml[(hq * NSPLIT + s) * 2 + 1] = l; }
  }
}

__global__ void __launch_bounds__(128) k_comb(const float* po, const float* pml, float* o) {
  int hq = blockIdx.x, d = threadIdx.x;
  float M = -INFINITY;
  for (int s = 0; s < NSPLIT; ++s) M = fmaxf(M, pml[(hq * NSPLIT + s) * 2]);
  float L = 0.f, acc = 0.f;
  for (int s = 0; s < NSPLIT; ++s) { float w = __expf(pml[(hq * NSPLIT + s) * 2] - M); L += w * pml[(hq * NSPLIT + s) * 2 + 1]; acc += w * po[((size_t)hq * NSPLIT + s) * DH + d]; }
  o[hq * DH + d] = rbf(acc / L);
}

__global__ void __launch_bounds__(256) k_wo(const float* o, const u16* wo, const u16* x, float* h) {
  __shared__ float part[256];
  int tid = threadIdx.x;
  int col = blockIdx.x * 16 + (tid & 15), kq = tid >> 4;
  float acc = 0.f;
  for (int k = kq * 256; k < kq * 256 + 256; ++k) acc += o[k] * bf2f(wo[(size_t)k * D + col]);
  part[tid] = acc;
  __syncthreads();
  if (tid < 16) { float a = 0.f; for (int j = 0; j < 16; ++j) a += part[j * 16 + tid]; h[col] = rbf(bf2f(x[col]) + rbf(a)); }
}

__global__ void __launch_bounds__(256) k_w1(const float* h, const u16* w1, float* h1) {
  __shared__ float xs[D], xn[D], red[4], part[256];
  int tid = threadIdx.x;
  for (int i = tid; i < D; i += 256) xs[i] = h[i];
  __syncthreads();
  layernorm_block(xs, xn, red);
  int col = blockIdx.x * 64 + (tid & 63), kq = tid >> 6;
  float acc = 0.f;
  for (int k = kq * 144; k < kq * 144 + 144; ++k) acc += xn[k] * bf2f(w1[(size_t)k * F + col]);
  part[tid] = acc;
  __syncthreads();
  if (tid < 64) { float a = rbf(part[tid] + part[tid + 64] + part[tid + 128] + part[tid + 192]); h1[col] = rbf(a / (1.f + __expf(-a))); }
}

__global__ void __launch_bounds__(256) k_w2(const float* h1, const u16* w2, const float* h, u16* out) {
  __shared__ float part[256];
  int tid = threadIdx.x;
  int col = blockIdx.x * 16 + (tid & 15), kq = tid >> 4;
  float acc = 0.f;
  for (int k = kq * 288; k < kq * 288 + 288; ++k) acc += h1[k] * bf2f(w2[(size_t)k * D + col]);
  part[tid] = acc;
  __syncthreads();
  if (tid < 16) { float a = 0.f; for (int j = 0; j < 16; ++j) a += part[j * 16 + tid]; out[col] = f2bf(h[col] + rbf(a)); }
}

extern "C" void kernel_launch(void* const* d_in, const int* in_sizes, int n_in, void* d_out, int out_size, void* d_ws, size_t ws_size, hipStream_t stream) {
  const u16* x = (const u16*)d_in[0];
  const u16* kc = (const u16*)d_in[1];
  const u16* vc = (const u16*)d_in[2];
  const u16* wq = (const u16*)d_in[3];
  const u16* wo = (const u16*)d_in[4];
  const u16* w1 = (const u16*)d_in[6];
  const u16* w2 = (const u16*)d_in[7];
  float* ws = (float*)d_ws;
  float* q = ws; float* po = q + 4096; float* pml = po + 32 * 16 * 128; float* o = pml + 1024; float* h = o + 4096; float* h1 = h + 1024;
  k_q<<<64, 256, 0, stream>>>(x, wq, q);
  k_attn<<<256, 256, 0, stream>>>(q, kc, vc, po, pml);
  k_comb<<<32, 128, 0, stream>>>(po, pml, o);
  k_wo<<<36, 256, 0, stream>>>(o, wo, x, h);
  k_w1<<<72, 256, 0, stream>>>(h, w1, h1);
  k_w2<<<36, 256, 0, stream>>>(h1, w2, h, (u16*)d_out);
}
```

```cpp
#include <hip/hip_runtime.h>
#include <stdint.h>

typedef unsigned short u16;
typedef unsigned long long u64;
typedef unsigned u32x4 __attribute__((ext_vector_type(4)));
typedef unsigned u32x2 __attribute__((ext_vector_type(2)));
typedef float f32x4 __attribute__((ext_vector_type(4)));
typedef short s16x4 __attribute__((ext_vector_type(4)));
typedef short s16x8 __attribute__((ext_vector_type(8)));
typedef __bf16 bf16x8 __attribute__((ext_vector_type(8)));
typedef __bf16 bf16x2 __attribute__((ext_vector_type(2)));
typedef __amdgpu_buffer_rsrc_t rsrc_t;
#define LDS3 __attribute__((address_space(3)))
#define GAS __attribute__((address_space(1)))
typedef GAS u64 gu64;

#define D1 4
#define D2 2
#define D3 2
#define D5 20
#define NTS " nt"

constexpr int NT = 576;
#define SPINLOG 18
constexpr unsigned SPIN_MAX = 1u << SPINLOG;
constexpr unsigned WS_EPOCH = 0;
constexpr unsigned WS_QS = 4096;
constexpr unsigned WS_PO = WS_QS + 131072;
constexpr unsigned WS_ML = WS_PO + 524288;
constexpr unsigned WS_H3 = WS_ML + 8192;
constexpr unsigned WS_HB = WS_H3 + 516096;
constexpr unsigned WS_H5 = WS_HB + 8192;
constexpr unsigned WS_END = WS_H5 + 663552;
constexpr unsigned WS_STAMP = 8u << 20;
constexpr unsigned L_WQ = 0;
constexpr unsigned L_HN = 0;
constexpr unsigned L_ACT = 2048;
constexpr unsigned L_OB = 4096;
constexpr unsigned L_K = 18432;
constexpr unsigned L_V = L_K + 32768;
constexpr unsigned L_W = L_V + 32768;
constexpr unsigned L_W2 = L_W + 36864;
constexpr unsigned L_S = L_W2 + 36864;
constexpr unsigned L_PART = L_S;
constexpr unsigned L_Q = L_PART + 2304;
constexpr unsigned L_P = L_Q + 512;
constexpr unsigned L_MW = L_P + 512;
constexpr unsigned L_LW = L_MW + 64;
constexpr unsigned L_RED = L_LW + 64;
constexpr unsigned L_END = L_RED + 320;
static_assert(L_END <= 163840, "LDS");

__device__ __forceinline__ float bf2f(unsigned v16) { return __uint_as_float(v16 << 16); }
__device__ __forceinline__ float bflo(unsigned v) { return __uint_as_float(v << 16); }
__device__ __forceinline__ float bfhi(unsigned v) { return __uint_as_float(v & 0xFFFF0000u); }
__device__ __forceinline__ unsigned pack2(float a, float b) { bf16x2 v = {(__bf16)a, (__bf16)b}; return __builtin_bit_cast(unsigned, v); }
__device__ __forceinline__ float rbf(float f) { return bflo(pack2(f, f)); }
template <int CTRL> __device__ __forceinline__ float dpp(float x) {
  return __builtin_bit_cast(float, __builtin_amdgcn_mov_dpp(__builtin_bit_cast(int, x), CTRL, 0xf, 0xf, true));
}
__device__ __forceinline__ float x16_sum(float x) { auto s = __builtin_amdgcn_permlane16_swap(__float_as_uint(x), __float_as_uint(x), false, false); return __uint_as_float(s[0]) + __uint_as_float(s[1]); }
__device__ __forceinline__ float x32_sum(float x) { auto s = __builtin_amdgcn_permlane32_swap(__float_as_uint(x), __float_as_uint(x), false, false); return __uint_as_float(s[0]) + __uint_as_float(s[1]); }
__device__ __forceinline__ float x16_max(float x) { auto s = __builtin_amdgcn_permlane16_swap(__float_as_uint(x), __float_as_uint(x), false, false); return fmaxf(__uint_as_float(s[0]), __uint_as_float(s[1])); }
__device__ __forceinline__ float x32_max(float x) { auto s = __builtin_amdgcn_permlane32_swap(__float_as_uint(x), __float_as_uint(x), false, false); return fmaxf(__uint_as_float(s[0]), __uint_as_float(s[1])); }
__device__ __forceinline__ float row16_sum(float x) { x += dpp<0xB1>(x); x += dpp<0x4E>(x); x += dpp<0x141>(x); x += dpp<0x140>(x); return x; }
__device__ __forceinline__ float row16_max(float x) { x = fmaxf(x, dpp<0xB1>(x)); x = fmaxf(x, dpp<0x4E>(x)); x = fmaxf(x, dpp<0x141>(x)); x = fmaxf(x, dpp<0x140>(x)); return x; }
__device__ __forceinline__ float dot2(unsigned a, unsigned b, float c) { return __builtin_amdgcn_fdot2_f32_bf16(__builtin_bit_cast(bf16x2, a), __builtin_bit_cast(bf16x2, b), c, false); }
__device__ __forceinline__ float fexp(float x) { return __builtin_amdgcn_exp2f(x * 1.4426950408889634f); }

__device__ __forceinline__ void dma16(unsigned ldsaddr, unsigned voff, rsrc_t r, unsigned soff) {
  asm volatile("s_mov_b32 m0, %0\n\ts_nop 0\n\tbuffer_load_dwordx4 %1, %2, %3 offen" NTS " lds" ::"s"(ldsaddr), "v"(voff), "s"(r), "s"(soff) : "memory");
}
#define WAITVM(n) asm volatile("s_waitcnt vmcnt(" #n ")" ::: "memory")
#define BAR() asm volatile("s_waitcnt lgkmcnt(0)\n\ts_barrier" ::: "memory")
__device__ __forceinline__ void ld16_sc1_wait(u32x4& d, unsigned voff, rsrc_t r) {
  asm volatile("buffer_load_dwordx4 %0, %1, %2, 0 offen sc1\n\ts_waitcnt vmcnt(0)" : "=&v"(d) : "v"(voff), "s"(r) : "memory");
}
__device__ __forceinline__ void ld16x2_sc1_wait(u32x4& d0, u32x4& d1, unsigned voff0, unsigned voff1, rsrc_t r) {
  asm volatile("buffer_load_dwordx4 %0, %2, %4, 0 offen sc1\n\tbuffer_load_dwordx4 %1, %3, %4, 0 offen sc1\n\ts_waitcnt vmcnt(0)"
               : "=&v"(d0), "=&v"(d1) : "v"(voff0), "v"(voff1), "s"(r) : "memory");
}
__device__ __forceinline__ void ld8_sc1_wait(u32x2& d, unsigned voff, rsrc_t r) {
  asm volatile("buffer_load_dwordx2 %0, %1, %2, 0 offen sc1\n\ts_waitcnt vmcnt(0)" : "=&v"(d) : "v"(voff), "s"(r) : "memory");
}
struct Tag { unsigned th, t8; };
__device__ __forceinline__ void put(unsigned char* ws, unsigned off, float v, const Tag& t) {
  unsigned lo = ((__float_as_uint(v) + 0x80u) & 0xFFFFFF00u) | t.t8;
  __hip_atomic_store((gu64*)(ws + off), ((u64)t.th << 32) | lo, __ATOMIC_RELAXED, __HIP_MEMORY_SCOPE_AGENT);
}
__device__ __forceinline__ unsigned bad(unsigned lo, unsigned hi, const Tag& t) { return (hi ^ t.th) | ((lo ^ t.t8) & 0xFFu); }
__device__ __forceinline__ float pay(unsigned lo) { return __uint_as_float(lo & 0xFFFFFF00u); }
__device__ __forceinline__ bool allok(unsigned b) { return __builtin_amdgcn_ballot_w64(b != 0) == 0; }
template <typename T> __device__ __forceinline__ LDS3 T* lptr(unsigned a) { return (LDS3 T*)(uintptr_t)a; }
__device__ __forceinline__ s16x4 trd(unsigned a) { return __builtin_amdgcn_ds_read_tr16_b64_v4i16(lptr<s16x4>(a)); }
__device__ __forceinline__ bf16x8 cat(s16x4 a, s16x4 b) { s16x8 v = __builtin_shufflevector(a, b, 0, 1, 2, 3, 4, 5, 6, 7); return __builtin_bit_cast(bf16x8, v); }
__device__ __forceinline__ f32x4 mfma32(bf16x8 a, bf16x8 b, f32x4 c) { return __builtin_amdgcn_mfma_f32_16x16x32_bf16(a, b, c, 0, 0, 0); }
__device__ __forceinline__ f32x4 mfma16(s16x4 a, s16x4 b, f32x4 c) { return __builtin_amdgcn_mfma_f32_16x16x16bf16_1k(a, b, c, 0, 0, 0); }
__device__ __forceinline__ float sel4(int rg, float a, float b, float c, float d) { float x = rg & 1 ? b : a, y = rg & 1 ? d : c; return rg & 2 ? y : x; }

#define ST(i)
#define STDUMP()

__global__ void __launch_bounds__(NT, 3)
fused_decode_kernel(const u16* __restrict__ x, const u16* __restrict__ kc, const u16* __restrict__ vc, const u16* __restrict__ wq,
                    const u16* __restrict__ wo, const u16* __restrict__ w1, const u16* __restrict__ w2, u16* __restrict__ out,
                    unsigned char* __restrict__ ws) {
  __shared__ __attribute__((aligned(1024))) unsigned char lds[L_END];
  const int tid = threadIdx.x, l = tid & 63;
  int w = __builtin_amdgcn_readfirstlane(tid >> 6);
  asm volatile("s_nop 4" : "+s"(w));
  const int b = blockIdx.x, xcd = b & 7, r = b >> 3, g = xcd * 2 + (r >> 4), s = r & 15;
  const unsigned LB = (unsigned)(uintptr_t)(LDS3 unsigned char*)lds;
  const int n16 = l & 15, kg = l >> 4, q4 = (l >> 2) & 3, c4 = l & 3;
  ST(0);
  const rsrc_t rs_ws = __builtin_amdgcn_make_buffer_rsrc((void*)ws, 0, WS_END, 0x00020000);
  const rsrc_t rs_x = __builtin_amdgcn_make_buffer_rsrc((void*)x, 0, 1152, 0x00020000);
  const rsrc_t rs_wq = __builtin_amdgcn_make_buffer_rsrc((void*)wq, 0, 576 * 8192, 0x00020000);
  const size_t kvoff = ((size_t)g * 8192 + 6144 + s * 128) * 128;
  const rsrc_t rs_k = __builtin_amdgcn_make_buffer_rsrc((void*)(kc + kvoff), 0, 32768, 0x00020000);
  const rsrc_t rs_v = __builtin_amdgcn_make_buffer_rsrc((void*)(vc + kvoff), 0, 32768, 0x00020000);
  const int kq = s >> 2, cb = s & 3;

  asm volatile("" ::"s"(x), "s"(kc), "s"(vc), "s"(wq), "s"(wo), "s"(w1), "s"(w2), "s"(out), "s"(ws));
  u64 ep; u32x2 xo; u32x4 xa, xb;
  asm volatile("s_load_dwordx2 %0, %1, 0x0" : "=&s"(ep) : "s"(ws) : "memory");
  {
    unsigned o16 = l * 16, oo = (kq * 144 + w * 16 + kg * 4) * 2;
    asm volatile("buffer_load_dwordx4 %0, %3, %5, 0 offen\n\t"
                 "buffer_load_dwordx4 %1, %3, %5, 0 offen offset:1024\n\t"
                 "buffer_load_dwordx2 %2, %4, %5, 0 offen"
                 : "=&v"(xa), "=&v"(xb), "=&v"(xo) : "v"(o16), "v"(oo), "s"(rs_x) : "memory");
  }
  const unsigned sw128 = ((l & 7) ^ (2 * (l >> 4))) * 16 + (l >> 3) * 0;
  {
    unsigned voff = (l >> 3) * 8192 + sw128;
    unsigned so = (kq * 144 + w * 16) * 8192 + (g * 256 + cb * 64) * 2;
    dma16(LB + L_WQ + w * 2048, voff, rs_wq, so);
    dma16(LB + L_WQ + w * 2048 + 1024, voff, rs_wq, so + 8 * 8192);
  }
  if (w < 8) {
#pragma unroll
    for (int i = 0; i < 4; ++i) {
      unsigned voff = kg * 256 + ((n16 ^ (4 * i + kg)) * 16);
      dma16(LB + L_K + w * 4096 + i * 1024, voff, rs_k, (w * 16 + 4 * i) * 256);
    }
  }
  asm volatile("s_cmp_lt_u32 %4, 8\n\ts_cbranch_scc1 .Lxw%=\n\ts_waitcnt vmcnt(2)\n.Lxw%=:\n\ts_waitcnt vmcnt(6) lgkmcnt(0)"
               : "+v"(xa), "+v"(xb), "+v"(xo), "+s"(ep) : "s"(w) : "scc", "memory");
  ST(1);
  Tag tag;
  const u64 epoch = ep + 1;
  tag.th = (unsigned)(epoch >> 8); tag.t8 = (unsigned)epoch & 0xFFu;
  s16x4 afq;
  {
    float s1 = 0.f, s2 = 0.f, t1 = 0.f, t2 = 0.f;
#pragma unroll
    for (int i = 0; i < 4; ++i) { s1 = dot2(xa[i], 0x3F803F80u, s1); s2 = dot2(xa[i], xa[i], s2); t1 = dot2(xb[i], 0x3F803F80u, t1); t2 = dot2(xb[i], xb[i], t2); }
    s1 += t1; s2 += t2;
    s1 = x32_sum(x16_sum(row16_sum(s1))); s2 = x32_sum(x16_sum(row16_sum(s2)));
    float mean = s1 * (1.f / 576.f), var = fmaxf(s2 * (1.f / 576.f) - mean * mean, 0.f);
    float mb = rbf(mean), ri = __builtin_amdgcn_rcpf(rbf(__builtin_amdgcn_sqrtf(rbf(rbf(var) + 1e-5f))));
    u32x2 a;
    a.x = pack2(rbf(bflo(xo.x) - mb) * ri, rbf(bfhi(xo.x) - mb) * ri);
    a.y = pack2(rbf(bflo(xo.y) - mb) * ri, rbf(bfhi(xo.y) - mb) * ri);
    afq = __builtin_bit_cast(s16x4, a);
  }
  if (w < 8) WAITVM(4); else WAITVM(0);
  ST(2);
  {
    const int row = 4 * kg + q4;
    const unsigned a0 = LB + L_WQ + w * 2048 + row * 128 + (((c4 >> 1) ^ (2 * ((row >> 1) & 3))) * 16) + (c4 & 1) * 8;
    f32x4 z4 = {0.f, 0.f, 0.f, 0.f};
    f32x4 d0 = mfma16(afq, trd(a0), z4), d1 = mfma16(afq, trd(a0 ^ 32), z4), d2 = mfma16(afq, trd(a0 ^ 64), z4), d3 = mfma16(afq, trd(a0 ^ 96), z4);
    *lptr<float>(LB + L_PART + (w * 64 + l) * 4) = sel4(kg, d0[0], d1[0], d2[0], d3[0]);
  }
  BAR();
  if (w == 8) {
    float a = 0.f;
#pragma unroll
    for (int i = 0; i < 9; ++i) a += *lptr<float>(LB + L_PART + (i * 64 + l) * 4);
    put(ws, WS_QS + ((g * 4 + kq) * 256 + cb * 64 + l) * 8, a, tag);
  }
  ST(3);
  asm volatile("s_barrier" ::: "memory");
  if (w < 4) {
#pragma unroll
    for (int i = 0; i < 8; ++i) {
      unsigned voff = kg * 256 + ((n16 ^ (2 * kg) ^ (8 * (i & 1))) * 16);
      dma16(LB + L_V + w * 8192 + i * 1024, voff, rs_v, (w * 32 + 4 * i) * 256);
    }
  } else if (w < 8) {
    for (int i = 0; i < D1; ++i) __builtin_amdgcn_s_sleep(1);
    const unsigned o0 = WS_QS + ((g * 4 + kg) * 256 + (w - 4) * 64 + 2 * n16) * 8;
    u32x4 a, c;
    for (unsigned it = 0; it < SPIN_MAX; ++it) {
      ld16x2_sc1_wait(a, c, o0, o0 + 256, rs_ws);
      if (allok(bad(a.x, a.y, tag) | bad(a.z, a.w, tag) | bad(c.x, c.y, tag) | bad(c.z, c.w, tag))) break;
    }
    float q0 = x32_sum(x16_sum(pay(a.x))), q1 = x32_sum(x16_sum(pay(a.z))), q2 = x32_sum(x16_sum(pay(c.x))), q3 = x32_sum(x16_sum(pay(c.z)));
    if (l < 16) {
      *lptr<unsigned>(LB + L_Q + ((w - 4) * 64 + 2 * l) * 2) = pack2(q0, q1);
      *lptr<unsigned>(LB + L_Q + ((w - 4) * 64 + 32 + 2 * l) * 2) = pack2(q2, q3);
    }
  }
  ST(4);
  bf16x8 ka[4];
  if (w < 8) {
    if (w < 4) WAITVM(8);
    const unsigned a0 = LB + L_K + w * 4096 + n16 * 256 + ((kg ^ n16) * 16);
#pragma unroll
    for (int j = 0; j < 4; ++j) ka[j] = *lptr<bf16x8>(a0 ^ (j * 64));
  }
  BAR();
  ST(5);
  float sc[4], mloc = 0.f;
  if (w < 8) {
    f32x4 d = {0.f, 0.f, 0.f, 0.f};
    const unsigned a0 = LB + L_Q + (n16 & 1) * 256 + kg * 16;
#pragma unroll
    for (int j = 0; j < 4; ++j) d = mfma32(ka[j], *lptr<bf16x8>(a0 + j * 64), d);
#pragma unroll
    for (int i = 0; i < 4; ++i) sc[i] = rbf(rbf(d[i]) * 0.08838834764831845f);
    mloc = x32_max(x16_max(fmaxf(fmaxf(sc[0], sc[1]), fmaxf(sc[2], sc[3]))));
    if (l < 2) *lptr<float>(LB + L_MW + (l * 8 + w) * 4) = mloc;
  }
  BAR();
  float Mwg = 0.f;
  if (w < 8) {
    f32x4 m0 = *lptr<f32x4>(LB + L_MW + (n16 & 1) * 32), m1 = *lptr<f32x4>(LB + L_MW + (n16 & 1) * 32 + 16);
    Mwg = fmaxf(fmaxf(fmaxf(m0[0], m0[1]), fmaxf(m0[2], m0[3])), fmaxf(fmaxf(m1[0], m1[1]), fmaxf(m1[2], m1[3])));
    float p0 = fexp(sc[0] - Mwg), p1 = fexp(sc[1] - Mwg), p2 = fexp(sc[2] - Mwg), p3 = fexp(sc[3] - Mwg);
    float lw = x32_sum(x16_sum((p0 + p1) + (p2 + p3)));
    if (n16 < 2) {
      u32x2 pp = {pack2(p0, p1), pack2(p2, p3)};
      *lptr<u32x2>(LB + L_P + n16 * 256 + (32 * (w >> 1) + 8 * kg + 4 * (w & 1)) * 2) = pp;
    }
    if (l < 2) *lptr<float>(LB + L_LW + (l * 8 + w) * 4) = lw;
  }
  ST(6);
  const bool orole = s < 7;
  const int us = s < 4 ? 5 * s : 20 + 4 * (s - 4), nu = s < 4 ? 5 : 4;
  const int f = g * 9 + (s - 7);
  if (orole) {
    const rsrc_t rs_wo = __builtin_amdgcn_make_buffer_rsrc((void*)wo, 0, 4096 * 1152, 0x00020000);
    unsigned voff = (l >> 3) * 1152 + sw128;
    unsigned so = (g * 256 + us * 8) * 1152 + w * 128;
#pragma unroll
    for (int i = 0; i < 4; ++i) dma16(LB + L_W + w * 5120 + i * 1024, voff, rs_wo, so + i * 8 * 1152);
    if (nu == 5) { dma16(LB + L_W + w * 5120 + 4096, voff, rs_wo, so + 32 * 1152); if (w < 4) WAITVM(5); }
    else if (w < 4) WAITVM(4);
  } else {
    const rsrc_t rs_w1 = __builtin_amdgcn_make_buffer_rsrc((void*)w1, 0, 576 * 9216, 0x00020000);
    unsigned voff = (l >> 2) * 9216 + (((l & 3) ^ (2 * ((l >> 4) & 1))) * 16);
    unsigned so = (w * 64) * 9216 + f * 64;
#pragma unroll
    for (int i = 0; i < 4; ++i) dma16(LB + L_W + w * 4096 + i * 1024, voff, rs_w1, so + i * 16 * 9216);
    if (w < 4) WAITVM(4);
  }
  BAR();
  ST(7);
  if (w < 8) {
    const int key = 4 * kg + q4;
    const unsigned va = LB + L_V + key * 256 + (((2 * w + (c4 >> 1)) ^ (2 * (key & 7))) * 16) + (c4 & 1) * 8;
    const unsigned pa = LB + L_P + (n16 & 1) * 256 + kg * 16;
    f32x4 d = {0.f, 0.f, 0.f, 0.f}, e = {0.f, 0.f, 0.f, 0.f};
    d = mfma32(cat(trd(va), trd(va + 4096)), *lptr<bf16x8>(pa), d);
    e = mfma32(cat(trd(va + 8192), trd(va + 8192 + 4096)), *lptr<bf16x8>(pa + 64), e);
    d = mfma32(cat(trd(va + 16384), trd(va + 16384 + 4096)), *lptr<bf16x8>(pa + 128), d);
    e = mfma32(cat(trd(va + 24576), trd(va + 24576 + 4096)), *lptr<bf16x8>(pa + 192), e);
    d += e;
    if (n16 < 8) put(ws, WS_PO + ((g * 16 + s) * 256 + (n16 & 1) * 128 + 16 * w + 4 * kg + (n16 >> 1)) * 8, sel4(n16 >> 1, d[0], d[1], d[2], d[3]), tag);
    if (w == 0 && l < 4) {
      f32x4 l0 = *lptr<f32x4>(LB + L_LW + (l >> 1) * 32), l1 = *lptr<f32x4>(LB + L_LW + (l >> 1) * 32 + 16);
      float L = ((l0[0] + l0[1]) + (l0[2] + l0[3])) + ((l1[0] + l1[1]) + (l1[2] + l1[3]));
      f32x4 m0 = *lptr<f32x4>(LB + L_MW + (l >> 1) * 32), m1 = *lptr<f32x4>(LB + L_MW + (l >> 1) * 32 + 16);
      float M = fmaxf(fmaxf(fmaxf(m0[0], m0[1]), fmaxf(m0[2], m0[3])), fmaxf(fmaxf(m1[0], m1[1]), fmaxf(m1[2], m1[3])));
      put(ws, WS_ML + (((g * 16 + s) * 2 + (l >> 1)) * 2 + (l & 1)) * 8, (l & 1) ? L : M, tag);
    }
  }
  ST(8);
  if (orole) {
    const int op = g * 7 + s;
    if (w < nu) {
      for (int i = 0; i < D2; ++i) __builtin_amdgcn_s_sleep(1);
      const int rel = (us + w) * 8 + 2 * kg, head = ((us + w) * 8) >> 7;
      const unsigned o0 = WS_PO + ((g * 16 + n16) * 256 + rel) * 8, o1 = WS_ML + ((g * 16 + n16) * 2 + head) * 16;
      u32x4 a, c;
      for (unsigned it = 0; it < SPIN_MAX; ++it) {
        ld16x2_sc1_wait(a, c, o0, o1, rs_ws);
        if (allok(bad(a.x, a.y, tag) | bad(a.z, a.w, tag) | bad(c.x, c.y, tag) | bad(c.z, c.w, tag))) break;
      }
      float m = pay(c.x), M = row16_max(m), wg = fexp(m - M);
      float L = row16_sum(wg * pay(c.z)), o_0 = row16_sum(wg * pay(a.x)), o_1 = row16_sum(wg * pay(a.z));
      float ri = 1.f / L;
      if (n16 == 0) {
        const int row = w * 8 + 2 * kg;
        const int pos = row < 32 ? 8 * ((row >> 2) & 3) + 4 * (row >> 4) + (row & 3) : row;
        *lptr<unsigned>(LB + L_OB + pos * 2) = pack2(o_0 * ri, o_1 * ri);
      }
    }
    WAITVM(0);
    const int row = 4 * kg + q4;
    const unsigned a0 = LB + L_W + w * 5120 + row * 128 + (((c4 >> 1) ^ (2 * ((row >> 1) & 3))) * 16) + (c4 & 1) * 8;
    bf16x8 bf[4]; s16x4 bg[4];
#pragma unroll
    for (int nb = 0; nb < 4; ++nb) bf[nb] = cat(trd(a0 ^ (nb * 32)), trd((a0 ^ (nb * 32)) + 2048));
    if (nu == 5) {
      const int row2 = 4 * (kg & 1) + q4;
      const unsigned a2 = LB + L_W + w * 5120 + 4096 + row2 * 128 + (((c4 >> 1) ^ (2 * ((row2 >> 1) & 3))) * 16) + (c4 & 1) * 8;
#pragma unroll
      for (int nb = 0; nb < 4; ++nb) bg[nb] = trd(a2 ^ (nb * 32));
    }
    BAR();
    ST(9);
    {
      bf16x8 af = *lptr<bf16x8>(LB + L_OB + kg * 16);
      f32x4 d[4];
#pragma unroll
      for (int nb = 0; nb < 4; ++nb) d[nb] = mfma32(af, bf[nb], (f32x4){0.f, 0.f, 0.f, 0.f});
      if (nu == 5) {
        u32x2 t = *lptr<u32x2>(LB + L_OB + 64 + (kg & 1) * 8);
        if (kg >= 2) t = (u32x2){0u, 0u};
        s16x4 a2 = __builtin_bit_cast(s16x4, t);
#pragma unroll
        for (int nb = 0; nb < 4; ++nb) d[nb] = mfma16(a2, bg[nb], d[nb]);
      }
      const int col = w * 64 + l;
      put(ws, WS_H3 + (((col >> 3) * 112 + op) * 8 + (col & 7)) * 8, sel4(kg, d[0][0], d[1][0], d[2][0], d[3][0]), tag);
    }
    ST(10);
    const int rd = s * 16 + g;
    if (rd < 72) {
      if (w < 7) {
        for (int i = 0; i < D3; ++i) __builtin_amdgcn_s_sleep(1);
        const unsigned o0 = WS_H3 + rd * 112 * 64 + (w * 64 + l) * 16;
        u32x4 a;
        for (unsigned it = 0; it < SPIN_MAX; ++it) {
          ld16_sc1_wait(a, o0, rs_ws);
          if (allok(bad(a.x, a.y, tag) | bad(a.z, a.w, tag))) break;
        }
        float v0 = pay(a.x), v1 = pay(a.z);
        v0 += dpp<0x124>(v0); v1 += dpp<0x124>(v1); v0 += dpp<0x128>(v0); v1 += dpp<0x128>(v1);
        v0 = x32_sum(x16_sum(v0)); v1 = x32_sum(x16_sum(v1));
        if (l < 4) { *lptr<float>(LB + L_RED + (w * 8 + 2 * l) * 4) = v0; *lptr<float>(LB + L_RED + (w * 8 + 2 * l + 1) * 4) = v1; }
      }
      BAR();
      float hval = 0.f;
      if (w == 0) {
        float a = 0.f;
#pragma unroll
        for (int i = 0; i < 7; ++i) a += *lptr<float>(LB + L_RED + (i * 8 + (l & 7)) * 4);
        const int xl = rd & 63;
        unsigned x0 = __builtin_amdgcn_readlane(rd < 64 ? xa.x : xb.x, xl), x1 = __builtin_amdgcn_readlane(rd < 64 ? xa.y : xb.y, xl);
        unsigned x2 = __builtin_amdgcn_readlane(rd < 64 ? xa.z : xb.z, xl), x3 = __builtin_amdgcn_readlane(rd < 64 ? xa.w : xb.w, xl);
        unsigned xs = (l & 4) ? ((l & 2) ? x3 : x2) : ((l & 2) ? x1 : x0);
        hval = rbf(((l & 1) ? bfhi(xs) : bflo(xs)) + rbf(a));
        if (l < 8) put(ws, WS_HB + (rd * 8 + l) * 8, hval, tag);
      }
      ST(11);
      BAR();
      {
        for (int i = 0; i < D5; ++i) __builtin_amdgcn_s_sleep(1);
        const unsigned o0 = WS_H5 + rd * 144 * 64 + tid * 16;
        u32x4 a;
        for (unsigned it = 0; it < SPIN_MAX; ++it) {
          ld16_sc1_wait(a, o0, rs_ws);
          if (allok(bad(a.x, a.y, tag) | bad(a.z, a.w, tag))) break;
        }
        ST(12);
        float v0 = pay(a.x), v1 = pay(a.z);
        v0 += dpp<0x124>(v0); v1 += dpp<0x124>(v1); v0 += dpp<0x128>(v0); v1 += dpp<0x128>(v1);
        v0 = x32_sum(x16_sum(v0)); v1 = x32_sum(x16_sum(v1));
        if (l < 4) { *lptr<float>(LB + L_RED + (w * 8 + 2 * l) * 4) = v0; *lptr<float>(LB + L_RED + (w * 8 + 2 * l + 1) * 4) = v1; }
      }
      BAR();
      if (w == 0) {
        float a = 0.f;
#pragma unroll
        for (int i = 0; i < 9; ++i) a += *lptr<float>(LB + L_RED + (i * 8 + (l & 7)) * 4);
        float o = hval + rbf(a);
        float o2 = dpp<0xB1>(o);
        if (l < 8 && !(l & 1)) *(unsigned*)(out + rd * 8 + l) = pack2(o, o2);
        if (rd == 0 && l == 0) __hip_atomic_store((gu64*)(ws + WS_EPOCH), epoch, __ATOMIC_RELAXED, __HIP_MEMORY_SCOPE_AGENT);
      }
      ST(13);
    }
  } else {
    {
      const rsrc_t rs_w2 = __builtin_amdgcn_make_buffer_rsrc((void*)w2, 0, 4608 * 1152, 0x00020000);
      unsigned voff = (l >> 3) * 1152 + sw128;
      unsigned so = (f * 32) * 1152 + w * 128;
#pragma unroll
      for (int i = 0; i < 4; ++i) dma16(LB + L_W2 + w * 4096 + i * 1024, voff, rs_w2, so + i * 8 * 1152);
    }
    u32x2 hg;
    for (unsigned it = 0; it < SPIN_MAX; ++it) {
      ld8_sc1_wait(hg, WS_HB + tid * 8, rs_ws);
      if (allok(bad(hg.x, hg.y, tag))) break;
    }
    ST(9);
    const float hv = pay(hg.x);
    {
      float s1 = x32_sum(x16_sum(row16_sum(hv))), s2 = x32_sum(x16_sum(row16_sum(hv * hv)));
      if (l == 0) { *lptr<float>(LB + L_RED + w * 8) = s1; *lptr<float>(LB + L_RED + w * 8 + 4) = s2; }
    }
    bf16x8 b1[2][2], b2[4];
    {
      const unsigned a0 = LB + L_W + w * 4096 + (4 * kg + q4) * 64 + (((c4 >> 1) ^ (2 * (kg & 1))) * 16) + (c4 & 1) * 8;
#pragma unroll
      for (int ks = 0; ks < 2; ++ks)
#pragma unroll
        for (int nb = 0; nb < 2; ++nb) b1[ks][nb] = cat(trd((a0 ^ (nb * 32)) + ks * 2048), trd((a0 ^ (nb * 32)) + ks * 2048 + 1024));
      const int row = 4 * kg + q4;
      const unsigned a2 = LB + L_W2 + w * 4096 + row * 128 + (((c4 >> 1) ^ (2 * ((row >> 1) & 3))) * 16) + (c4 & 1) * 8;
#pragma unroll
      for (int nb = 0; nb < 4; ++nb) b2[nb] = cat(trd(a2 ^ (nb * 32)), trd((a2 ^ (nb * 32)) + 2048));
    }
    BAR();
    {
      float s1 = 0.f, s2 = 0.f;
      if (n16 < 9) { s1 = *lptr<float>(LB + L_RED + n16 * 8); s2 = *lptr<float>(LB + L_RED + n16 * 8 + 4); }
      s1 = row16_sum(s1); s2 = row16_sum(s2);
      float mean = s1 * (1.f / 576.f), var = fmaxf(s2 * (1.f / 576.f) - mean * mean, 0.f);
      float mb = rbf(mean), ri = __builtin_amdgcn_rcpf(rbf(__builtin_amdgcn_sqrtf(rbf(rbf(var) + 1e-5f))));
      const int y = tid & 31, pos = (tid & ~31) + 8 * ((y >> 2) & 3) + 4 * (y >> 4) + (y & 3);
      *lptr<u16>(LB + L_HN + pos * 2) = (u16)pack2(rbf(hv - mb) * ri, 0.f);
    }
    BAR();
    ST(10);
    {
      bf16x8 a0 = *lptr<bf16x8>(LB + L_HN + (w * 64 + 8 * kg) * 2), a1 = *lptr<bf16x8>(LB + L_HN + (w * 64 + 32 + 8 * kg) * 2);
      f32x4 z4 = {0.f, 0.f, 0.f, 0.f};
      f32x4 d0 = mfma32(a0, b1[0][0], z4), d1 = mfma32(a0, b1[0][1], z4);
      d0 = mfma32(a1, b1[1][0], d0); d1 = mfma32(a1, b1[1][1], d1);
      if (l < 32) *lptr<float>(LB + L_PART + (w * 32 + l) * 4) = (l & 16) ? d1[0] : d0[0];
    }
    BAR();
    {
      float a = 0.f;
#pragma unroll
      for (int i = 0; i < 9; ++i) a += *lptr<float>(LB + L_PART + (i * 32 + (l & 31)) * 4);
      a = rbf(a);
      float act = a * __builtin_amdgcn_rcpf(1.f + fexp(-a));
      const int y = l & 31, pos = 8 * ((y >> 2) & 3) + 4 * (y >> 4) + (y & 3);
      if (l < 32) *lptr<u16>(LB + L_ACT + w * 64 + pos * 2) = (u16)pack2(act, 0.f);
      asm volatile("s_waitcnt lgkmcnt(0)" ::: "memory");
      bf16x8 af = *lptr<bf16x8>(LB + L_ACT + w * 64 + kg * 16);
      f32x4 z4 = {0.f, 0.f, 0.f, 0.f};
      f32x4 d0 = mfma32(af, b2[0], z4), d1 = mfma32(af, b2[1], z4), d2 = mfma32(af, b2[2], z4), d3 = mfma32(af, b2[3], z4);
      const int col = w * 64 + l;
      put(ws, WS_H5 + (((col >> 3) * 144 + f) * 8 + (col & 7)) * 8, sel4(kg, d0[0], d1[0], d2[0], d3[0]), tag);
    }
    ST(11);
  }
  STDUMP();
}

extern "C" void kernel_launch(void* const* d_in, const int* in_sizes, int n_in, void* d_out, int out_size, void* d_ws, size_t ws_size,
                              hipStream_t stream) {
  fused_decode_kernel<<<256, NT, 0, stream>>>((const u16*)d_in[0], (const u16*)d_in[1], (const u16*)d_in[2], (const u16*)d_in[3],
                                              (const u16*)d_in[4], (const u16*)d_in[6], (const u16*)d_in[7], (u16*)d_out,
                                              (unsigned char*)d_ws);
}
```

```cpp
#include <hip/hip_runtime.h>
#include <stdint.h>

typedef unsigned short u16;
typedef unsigned long long u64;
typedef unsigned u32x4 __attribute__((ext_vector_type(4)));
typedef unsigned u32x2 __attribute__((ext_vector_type(2)));
typedef float f32x4 __attribute__((ext_vector_type(4)));
typedef float f32x2 __attribute__((ext_vector_type(2)));
typedef short s16x4 __attribute__((ext_vector_type(4)));
typedef short s16x8 __attribute__((ext_vector_type(8)));
typedef __bf16 bf16x8 __attribute__((ext_vector_type(8)));
typedef __bf16 bf16x2 __attribute__((ext_vector_type(2)));
typedef __amdgpu_buffer_rsrc_t rsrc_t;
#define LDS3 __attribute__((address_space(3)))
#define GAS __attribute__((address_space(1)))
typedef GAS u64 gu64;

#define D1 20
#define D2 15
#define D3 0
#define D4 0
#define D5 0
#define REPX 1
#define NREP 16
#define SBA 200
#define SBO 200
#define SBF 200
#define NTS " nt"
#define SPINLOG 18

constexpr int NT = 576;
constexpr unsigned SPIN_MAX = 1u << SPINLOG;
constexpr unsigned LSPIN_MAX = SPIN_MAX << 6;
constexpr unsigned WS_EPOCH = 0;
constexpr unsigned WS_QS = 4096;
constexpr unsigned WS_PO = WS_QS + 131072;
constexpr unsigned WS_ML = WS_PO + 524288;
constexpr unsigned WS_H3 = WS_ML + 8192;
constexpr unsigned WS_HB = WS_H3 + 516096;
constexpr unsigned HB_STRIDE = 4352;
constexpr unsigned WS_H5 = WS_HB + 16 * HB_STRIDE;
constexpr unsigned WS_END = WS_H5 + 663552;
constexpr unsigned WS_STAMP = 8u << 20;
constexpr unsigned L_WQ = 0;
constexpr unsigned L_PART = 0;
constexpr unsigned L_K = 18432;
constexpr unsigned L_V = L_K + 32768;
constexpr unsigned L_W = L_V + 32768;
constexpr unsigned L_W2 = L_W + 36864;
constexpr unsigned L_S = L_W2 + 36864;
constexpr unsigned L_XN = L_S;
constexpr unsigned L_Q = L_XN + 320;
constexpr unsigned L_P = L_Q + 512;
constexpr unsigned L_FL = L_P + 512;
constexpr unsigned F_WQ = L_FL, F_XN = L_FL + 4, F_K = L_FL + 8, F_Q = L_FL + 12, F_V = L_FL + 16, F_P = L_FL + 20, F_W1 = L_FL + 24, F_W = L_FL + 28;
constexpr unsigned L_MW = L_FL + 32;
constexpr unsigned L_LW = L_MW + 64;
constexpr unsigned L_OB = L_LW + 64;
constexpr unsigned L_END = L_OB + 128;
static_assert(L_END <= 163840, "LDS");

__device__ __forceinline__ float bflo(unsigned v) { return __uint_as_float(v << 16); }
__device__ __forceinline__ float bfhi(unsigned v) { return __uint_as_float(v & 0xFFFF0000u); }
__device__ __forceinline__ unsigned pack2(float a, float b) { bf16x2 v = {(__bf16)a, (__bf16)b}; return __builtin_bit_cast(unsigned, v); }
__device__ __forceinline__ float rbf(float f) { return bflo(pack2(f, f)); }
template <int CTRL> __device__ __forceinline__ unsigned dppu(unsigned x) { return (unsigned)__builtin_amdgcn_mov_dpp((int)x, CTRL, 0xf, 0xf, true); }
template <int CTRL> __device__ __forceinline__ float dpp(float x) { return __uint_as_float(dppu<CTRL>(__float_as_uint(x))); }
__device__ __forceinline__ float x16_sum(float x) { auto s = __builtin_amdgcn_permlane16_swap(__float_as_uint(x), __float_as_uint(x), false, false); return __uint_as_float(s[0]) + __uint_as_float(s[1]); }
__device__ __forceinline__ float x32_sum(float x) { auto s = __builtin_amdgcn_permlane32_swap(__float_as_uint(x), __float_as_uint(x), false, false); return __uint_as_float(s[0]) + __uint_as_float(s[1]); }
__device__ __forceinline__ float x16_max(float x) { auto s = __builtin_amdgcn_permlane16_swap(__float_as_uint(x), __float_as_uint(x), false, false); return fmaxf(__uint_as_float(s[0]), __uint_as_float(s[1])); }
__device__ __forceinline__ float x32_max(float x) { auto s = __builtin_amdgcn_permlane32_swap(__float_as_uint(x), __float_as_uint(x), false, false); return fmaxf(__uint_as_float(s[0]), __uint_as_float(s[1])); }
__device__ __forceinline__ float row16_sum(float x) { x += dpp<0xB1>(x); x += dpp<0x4E>(x); x += dpp<0x141>(x); x += dpp<0x140>(x); return x; }
__device__ __forceinline__ float row16_max(float x) { x = fmaxf(x, dpp<0xB1>(x)); x = fmaxf(x, dpp<0x4E>(x)); x = fmaxf(x, dpp<0x141>(x)); x = fmaxf(x, dpp<0x140>(x)); return x; }
__device__ __forceinline__ float dot2(unsigned a, unsigned b, float c) { return __builtin_amdgcn_fdot2_f32_bf16(__builtin_bit_cast(bf16x2, a), __builtin_bit_cast(bf16x2, b), c, false); }
__device__ __forceinline__ float fexp(float x) { return __builtin_amdgcn_exp2f(x * 1.4426950408889634f); }

__device__ __forceinline__ void dma1(unsigned ldsaddr, unsigned voff, rsrc_t r, unsigned soff) {
  asm volatile("s_mov_b32 m0, %0\n\ts_nop 0\n\tbuffer_load_dwordx4 %1, %2, %3 offen" NTS " lds" ::"s"(ldsaddr), "v"(voff), "s"(r), "s"(soff) : "memory");
}
__device__ __forceinline__ void dma4c(unsigned ldsaddr, unsigned v0, unsigned v1, unsigned v2, unsigned v3, rsrc_t r, unsigned soff) {
  asm volatile("s_mov_b32 m0, %0\n\ts_nop 0\n\t"
               "buffer_load_dwordx4 %1, %5, %6 offen" NTS " lds\n\t"
               "buffer_load_dwordx4 %2, %5, %6 offen offset:1024" NTS " lds\n\t"
               "buffer_load_dwordx4 %3, %5, %6 offen offset:2048" NTS " lds\n\t"
               "buffer_load_dwordx4 %4, %5, %6 offen offset:3072" NTS " lds"
               ::"s"(ldsaddr), "v"(v0), "v"(v1), "v"(v2), "v"(v3), "s"(r), "s"(soff) : "memory");
}
#define WAITVM(n) asm volatile("s_waitcnt vmcnt(" #n ")" ::: "memory")
#define BAR() asm volatile("s_waitcnt lgkmcnt(0)\n\ts_barrier" ::: "memory")
template <int N> __device__ __forceinline__ void waitvm() { asm volatile("s_waitcnt vmcnt(%0)" ::"n"(N) : "memory"); }
template <int V> struct IC { static constexpr int v = V; };
template <int I, int N, class F> __device__ __forceinline__ void sfor(F&& f) { if constexpr (I < N) { f(IC<I>{}); sfor<I + 1, N>(f); } }
template <bool F> constexpr int uallow(int n, int gs, int sb) {
  int u = 0, t = gs;
  while (u < n && u < 59) { int i = n - 1 - u, c = (F && i >= 82 && i < 118) ? 16 : 8; if (t + c > sb) break; t += c; ++u; }
  return u;
}
constexpr int prevn(int n) { return n <= 18 ? 0 : n - 4; }
#define PIN(x) asm volatile("" : "+v"(x))
#define PINS(x) asm volatile("" : "+s"(x))
__device__ __forceinline__ void ld16_sc1_wait(u32x4& d, unsigned voff, rsrc_t r) {
  asm volatile("buffer_load_dwordx4 %0, %1, %2, 0 offen sc1\n\ts_waitcnt vmcnt(0)" : "=&v"(d) : "v"(voff), "s"(r) : "memory");
}
__device__ __forceinline__ void ld16x2_sc1_wait(u32x4& d0, u32x4& d1, unsigned voff0, unsigned voff1, rsrc_t r) {
  asm volatile("buffer_load_dwordx4 %0, %2, %4, 0 offen sc1\n\tbuffer_load_dwordx4 %1, %3, %4, 0 offen sc1\n\ts_waitcnt vmcnt(0)"
               : "=&v"(d0), "=&v"(d1) : "v"(voff0), "v"(voff1), "s"(r) : "memory");
}
struct Tag { unsigned th, t8; };
__device__ __forceinline__ void put(unsigned char* ws, unsigned off, float v, const Tag& t) {
  unsigned lo = ((__float_as_uint(v) + 0x80u) & 0xFFFFFF00u) | t.t8;
  __hip_atomic_store((gu64*)(ws + off), ((u64)t.th << 32) | lo, __ATOMIC_RELAXED, __HIP_MEMORY_SCOPE_AGENT);
}
__device__ __forceinline__ unsigned bad(unsigned lo, unsigned hi, const Tag& t) { return (hi ^ t.th) | ((lo ^ t.t8) & 0xFFu); }
__device__ __forceinline__ float pay(unsigned lo) { return __uint_as_float(lo & 0xFFFFFF00u); }
__device__ __forceinline__ bool allok(unsigned b) { return __builtin_amdgcn_ballot_w64(b != 0) == 0; }
template <typename T> __device__ __forceinline__ LDS3 T* lptr(unsigned a) { return (LDS3 T*)(uintptr_t)a; }
__device__ __forceinline__ s16x4 trd(unsigned a) { return __builtin_amdgcn_ds_read_tr16_b64_v4i16(lptr<s16x4>(a)); }
__device__ __forceinline__ bf16x8 cat(s16x4 a, s16x4 b) { s16x8 v = __builtin_shufflevector(a, b, 0, 1, 2, 3, 4, 5, 6, 7); return __builtin_bit_cast(bf16x8, v); }
__device__ __forceinline__ f32x4 mfma32(bf16x8 a, bf16x8 b, f32x4 c) { return __builtin_amdgcn_mfma_f32_16x16x32_bf16(a, b, c, 0, 0, 0); }
__device__ __forceinline__ f32x4 mfma16(s16x4 a, s16x4 b, f32x4 c) { return __builtin_amdgcn_mfma_f32_16x16x16bf16_1k(a, b, c, 0, 0, 0); }
__device__ __forceinline__ float sel4(int rg, float a, float b, float c, float d) { float x = rg & 1 ? b : a, y = rg & 1 ? d : c; return rg & 2 ? y : x; }
__device__ __forceinline__ void flag_set(unsigned a) { unsigned one = 1; asm volatile("ds_write_b32 %0, %1" ::"v"(a), "v"(one) : "memory"); }
__device__ __forceinline__ void flag_add(unsigned a) { unsigned one = 1; asm volatile("ds_add_u32 %0, %1" ::"v"(a), "v"(one) : "memory"); }
__device__ __forceinline__ void spin1(unsigned a) {
  for (unsigned it = 0; it < LSPIN_MAX; ++it) {
    unsigned v;
    asm volatile("ds_read_b32 %0, %1\n\ts_waitcnt lgkmcnt(0)" : "=v"(v) : "v"(a) : "memory");
    if (__builtin_amdgcn_readfirstlane(v) != 0) break;
  }
}
__device__ __forceinline__ void spin2(unsigned a, unsigned n1) {
  for (unsigned it = 0; it < LSPIN_MAX; ++it) {
    u32x2 v;
    asm volatile("ds_read_b64 %0, %1\n\ts_waitcnt lgkmcnt(0)" : "=v"(v) : "v"(a) : "memory");
    if (__builtin_amdgcn_readfirstlane(v.x == 0 ? 0u : v.y) == n1) break;
  }
}

#define ST(i)
#define STDUMP()

__global__ void __launch_bounds__(NT, 3)
fused_decode_kernel(const u16* __restrict__ x, const u16* __restrict__ kc, const u16* __restrict__ vc, const u16* __restrict__ wq,
                    const u16* __restrict__ wo, const u16* __restrict__ w1, const u16* __restrict__ w2, u16* __restrict__ out,
                    unsigned char* __restrict__ ws) {
  __shared__ __attribute__((aligned(1024))) unsigned char lds[L_END];
  const int tid = threadIdx.x, l = tid & 63;
  int w = __builtin_amdgcn_readfirstlane(tid >> 6);
  asm volatile("s_nop 4" : "+s"(w));
  const int b = blockIdx.x, xcd = b & 7, r = b >> 3, g = xcd * 2 + (r >> 4), s = r & 15;
  const unsigned LB = (unsigned)(uintptr_t)(LDS3 unsigned char*)lds;
  const int n16 = l & 15, kg = l >> 4, q4 = (l >> 2) & 3, c4 = l & 3;
  ST(0);
  if (w == 0 && l < 24) *(volatile LDS3 unsigned*)(uintptr_t)(LB + L_FL + l * 4) = l < 8 ? 0u : 0xFFFFFFFFu;
  BAR();
  asm volatile("" ::"s"(x), "s"(kc), "s"(vc), "s"(wq), "s"(wo), "s"(w1), "s"(w2), "s"(out), "s"(ws));
#define LOAD_EPOCH() (*(volatile const __attribute__((address_space(4))) u64*)(uintptr_t)ws)
  const rsrc_t rs_ws = __builtin_amdgcn_make_buffer_rsrc((void*)ws, 0, WS_END, 0x00020000);
  const int kq = s >> 2, cb = s & 3;
  const bool orole = s < 7;
  const int us = s < 4 ? 5 * s : 20 + 4 * (s - 4), nu = s < 4 ? 5 : 4;
  const int f = g * 9 + (s - 7);
  const int rd = s * 16 + g;
  Tag tag;
  u64 epoch;
  const unsigned trx = (2 * (((q4 >> 1) & 1) | ((kg & 1) << 1)));
  unsigned xr = 0;

  if (w == 8) {
    __builtin_amdgcn_s_setprio(3);
    const rsrc_t rs_wq = __builtin_amdgcn_make_buffer_rsrc((void*)wq, 0, 576 * 8192, 0x00020000);
    const size_t kvoff = ((size_t)g * 8192 + 6144 + s * 128) * 128;
    const rsrc_t rs_k = __builtin_amdgcn_make_buffer_rsrc((void*)(kc + kvoff), 0, 32768, 0x00020000);
    const rsrc_t rs_v = __builtin_amdgcn_make_buffer_rsrc((void*)(vc + kvoff), 0, 32768, 0x00020000);
    const unsigned sw0 = ((l & 7) ^ (2 * ((l >> 4) & 1))) * 16;
    {
      const unsigned vq0 = (l >> 3) * 8192 + sw0, vq1 = vq0 ^ 64;
      const unsigned so = (kq * 144) * 8192 + (g * 256 + cb * 64) * 2;
#pragma unroll
      for (int i = 0; i < 18; ++i) dma1(LB + L_WQ + i * 1024, (i & 1) ? vq1 : vq0, rs_wq, so + i * 65536);
    }
    ST(1);
    const u64 ep = LOAD_EPOCH();
    unsigned fl = LB + L_FL;
    PIN(fl);
#define GATE(F, n, gs, SB) do { \
      constexpr int u_ = uallow<F>(n, gs, SB), pn_ = prevn(n), pu_ = uallow<F>(pn_, 32, SB); \
      constexpr int t1_ = F ? 118 : 1000; \
      if constexpr ((n) - u_ >= 18 && ((n) == 18 || pn_ - pu_ < 18)) { waitvm<((n) - 18 > 63 ? 63 : (n) - 18)>(); if (l == 0) flag_set(fl + (F_WQ - L_FL)); ST(3); } \
      if constexpr ((n) - u_ >= 50 && pn_ - pu_ < 50) { waitvm<((n) - 50 > 63 ? 63 : (n) - 50)>(); if (l == 0) flag_set(fl + (F_K - L_FL)); ST(5); } \
      if constexpr ((n) - u_ >= 82 && pn_ - pu_ < 82) { waitvm<((n) - 82 > 63 ? 63 : (n) - 82)>(); if (l == 0) flag_set(fl + (F_V - L_FL)); ST(6); } \
      if constexpr ((n) - u_ >= t1_ && pn_ - pu_ < t1_) { waitvm<((n) - t1_ > 63 ? 63 : ((n) - t1_ < 0 ? 0 : (n) - t1_))>(); if (l == 0) flag_set(fl + (F_W1 - L_FL)); ST(4); } \
      if constexpr (u_ < (n)) waitvm<u_>(); } while (0)
    {
      const unsigned vk0 = kg * 256 + ((n16 ^ kg) * 16);
      sfor<0, 8>([&](auto T) { constexpr int t = T.v; GATE(false, 18 + 4 * t, 32, SBA); dma4c(LB + L_K + t * 4096, vk0, vk0 ^ 64, vk0 ^ 128, vk0 ^ 192, rs_k, t * 4096); });
    }
    {
      const unsigned vv0 = kg * 256 + ((n16 ^ (2 * kg)) * 16), vv1 = vv0 ^ 128;
      sfor<0, 8>([&](auto T) { constexpr int t = T.v; GATE(false, 50 + 4 * t, 32, SBA); dma4c(LB + L_V + t * 4096, vv0, vv1, vv0, vv1, rs_v, t * 4096); });
    }
    const unsigned vw0 = (l >> 3) * 1152 + sw0, vw1 = vw0 ^ 64;
#define WTILE(rs, wl, wst, wso, t) do { const unsigned m_ = (wl) + (t) * (wst), s_ = (wso) + (t) * 128; \
      dma1(m_, vw0, rs, s_); dma1(m_ + 1024, vw1, rs, s_ + 9216); dma1(m_ + 2048, vw0, rs, s_ + 18432); dma1(m_ + 3072, vw1, rs, s_ + 27648); } while (0)
    if (orole) {
      const rsrc_t rs_o = __builtin_amdgcn_make_buffer_rsrc((void*)wo, 0, 4096 * 1152, 0x00020000);
      const unsigned wso = (g * 256 + us * 8) * 1152;
      sfor<0, 9>([&](auto T) { constexpr int t = T.v; GATE(false, 82 + 4 * t, 32, SBO); WTILE(rs_o, LB + L_W, 5120, wso, t); });
      if (nu == 5) {
        waitvm<uallow<false>(118, 72, SBO)>();
#pragma unroll
        for (int t = 0; t < 9; ++t) dma1(LB + L_W + t * 5120 + 4096, vw0, rs_o, wso + t * 128 + 36864);
      }
    } else {
      const rsrc_t rs_w1 = __builtin_amdgcn_make_buffer_rsrc((void*)w1, 0, 576 * 9216, 0x00020000);
      const rsrc_t rs_w2 = __builtin_amdgcn_make_buffer_rsrc((void*)w2, 0, 4608 * 1152, 0x00020000);
      const unsigned v1 = (l >> 2) * 9216 + (((l & 3) ^ (2 * ((l >> 5) & 1))) * 16);
      sfor<0, 9>([&](auto T) { constexpr int t = T.v; GATE(true, 82 + 4 * t, 64, SBF);
        const unsigned m_ = LB + L_W + t * 4096, s_ = (t * 64) * 9216 + f * 64;
        dma1(m_, v1, rs_w1, s_); dma1(m_ + 1024, v1, rs_w1, s_ + 16 * 9216); dma1(m_ + 2048, v1, rs_w1, s_ + 32 * 9216); dma1(m_ + 3072, v1, rs_w1, s_ + 48 * 9216); });
      const unsigned wso = f * 32 * 1152;
      sfor<0, 9>([&](auto T) { constexpr int t = T.v; GATE(true, 118 + 4 * t, 32, SBF); WTILE(rs_w2, LB + L_W2, 4096, wso, t); });
    }
    ST(7);
    waitvm<0>();
    if (l == 0) { flag_set(fl + (F_WQ - L_FL)); flag_set(fl + (F_K - L_FL)); flag_set(fl + (F_V - L_FL)); flag_set(fl + (F_W1 - L_FL)); flag_set(fl + (F_W - L_FL)); }
    __builtin_amdgcn_s_setprio(0);
    epoch = ep + 1; tag.th = (unsigned)(epoch >> 8); tag.t8 = (unsigned)epoch & 0xFFu;
    ST(8);
  } else {
    const rsrc_t rs_x = __builtin_amdgcn_make_buffer_rsrc((void*)x, 0, 1152, 0x00020000);
    u32x4 xa, xb, xq;
    if (w == 7) {
      unsigned o16 = l * 16, oq = kq * 288 + l * 16;
      asm volatile("buffer_load_dwordx4 %0, %3, %5, 0 offen\n\t"
                   "buffer_load_dwordx4 %1, %3, %5, 0 offen offset:1024\n\t"
                   "buffer_load_dwordx4 %2, %4, %5, 0 offen"
                   : "=&v"(xa), "=&v"(xb), "=&v"(xq) : "v"(o16), "v"(oq), "s"(rs_x) : "memory");
    }
    if (w == 0) {
      unsigned o = (rd * 8 + (l & 7)) * 2;
      asm volatile("buffer_load_ushort %0, %1, %2, 0 offen" : "=&v"(xr) : "v"(o), "s"(rs_x) : "memory");
    }
    const u64 ep = LOAD_EPOCH();
    unsigned a_wq = LB + L_WQ + (8 * kg + q4) * 128 + (((2 * (w & 3) + (c4 >> 1)) ^ trx) * 16) + (c4 & 1) * 8;
    unsigned a_wqt = LB + L_WQ + (128 + 8 * (kg & 1) + 4 * (kg >> 1) + q4) * 128 + (((2 * (w & 3) + (c4 >> 1)) ^ trx) * 16) + (c4 & 1) * 8;
    unsigned a_xn = LB + L_XN + kg * 16, a_xnt = LB + L_XN + 256 + 16 * (kg & 1) + 8 * (kg >> 1);
    unsigned o_qp = WS_QS + ((g * 4 + kq) * 256 + cb * 64 + (w & 3) * 16 + n16) * 8;
    unsigned o_qg = WS_QS + (g * 4 * 256 + (w & 3) * 64 + l) * 8;
    unsigned a_qw = LB + L_Q + ((w & 3) * 64 + l) * 2;
    unsigned a_k = LB + L_K + w * 4096 + n16 * 256 + ((kg ^ n16) * 16);
    unsigned a_q = LB + L_Q + (n16 & 1) * 256 + kg * 16;
    unsigned a_mw = LB + L_MW + ((l & 1) * 8 + w) * 4;
    unsigned a_mr = LB + L_MW + (n16 & 1) * 32;
    unsigned a_pw = LB + L_P + (n16 & 1) * 256 + (32 * (w >> 1) + 8 * kg + 4 * (w & 1)) * 2;
    unsigned a_v = LB + L_V + (4 * kg + q4) * 256 + (((2 * w + (c4 >> 1)) ^ (2 * ((4 * kg + q4) & 7))) * 16) + (c4 & 1) * 8;
    unsigned a_p = LB + L_P + (n16 & 1) * 256 + kg * 16;
    unsigned o_po = WS_PO + ((g * 16 + s) * 256 + (n16 & 1) * 128 + 16 * w + 4 * kg + (n16 >> 1)) * 8;
    unsigned a_lw = LB + L_LW + n16 * 4;
    unsigned o_ml = WS_ML + (((g * 16 + s) * 2 + ((l & 7) == 0 ? (l >> 3) : (l & 1))) * 2 + ((l & 7) == 0 ? 1 : 0)) * 8;
    unsigned fl = LB + L_FL;
    PIN(a_wq); PIN(a_wqt); PIN(a_xn); PIN(a_xnt); PIN(o_qp); PIN(o_qg); PIN(a_qw); PIN(a_k); PIN(a_q); PIN(a_mw); PIN(a_mr); PIN(a_pw); PIN(a_v); PIN(a_p); PIN(o_po);
    PIN(a_lw); PIN(o_ml); PIN(fl);
    if (w == 7) {
      asm volatile("s_waitcnt vmcnt(0)" : "+v"(xa), "+v"(xb), "+v"(xq)::"memory");
      ST(1);
      float s1 = 0.f, s2 = 0.f, t1 = 0.f, t2 = 0.f;
#pragma unroll
      for (int i = 0; i < 4; ++i) { s1 = dot2(xa[i], 0x3F803F80u, s1); s2 = dot2(xa[i], xa[i], s2); t1 = dot2(xb[i], 0x3F803F80u, t1); t2 = dot2(xb[i], xb[i], t2); }
      s1 += t1; s2 += t2;
      s1 = x32_sum(x16_sum(row16_sum(s1))); s2 = x32_sum(x16_sum(row16_sum(s2)));
      float mean = s1 * (1.f / 576.f), var = fmaxf(s2 * (1.f / 576.f) - mean * mean, 0.f);
      float mb = rbf(mean), ri = __builtin_amdgcn_rcpf(rbf(__builtin_amdgcn_sqrtf(rbf(rbf(var) + 1e-5f))));
      u32x4 a;
#pragma unroll
      for (int i = 0; i < 4; ++i) a[i] = pack2(rbf(bflo(xq[i]) - mb) * ri, rbf(bfhi(xq[i]) - mb) * ri);
      if (l < 18) *lptr<u32x4>(LB + L_XN + l * 16) = a;
      if (l == 0) flag_set(fl + (F_XN - L_FL));
    }
    if (w == 0) asm volatile("s_waitcnt vmcnt(0)" : "+v"(xr)::"memory");
    epoch = ep + 1; tag.th = (unsigned)(epoch >> 8); tag.t8 = (unsigned)epoch & 0xFFu;
    PINS(tag.th); PINS(tag.t8);
    ST(2);
    if (w < 4) {
      spin2(fl + (F_WQ - L_FL), 1);
      ST(3);
      f32x4 z4 = {0.f, 0.f, 0.f, 0.f};
      bf16x8 a0 = *lptr<bf16x8>(a_xn), a1 = *lptr<bf16x8>(a_xn + 64), a2 = *lptr<bf16x8>(a_xn + 128), a3 = *lptr<bf16x8>(a_xn + 192);
      s16x4 at = *lptr<s16x4>(a_xnt);
      bf16x8 b0 = cat(trd(a_wq), trd(a_wq + 512)), b1 = cat(trd(a_wq + 4096), trd(a_wq + 4096 + 512));
      bf16x8 b2 = cat(trd(a_wq + 8192), trd(a_wq + 8192 + 512)), b3 = cat(trd(a_wq + 12288), trd(a_wq + 12288 + 512));
      s16x4 bt = trd(a_wqt);
      f32x4 d = mfma32(a0, b0, z4), e = mfma32(a1, b1, z4);
      d = mfma32(a2, b2, d); e = mfma32(a3, b3, e);
      d = mfma16(at, bt, d);
      if (l < 16) put(ws, o_qp, d[0] + e[0], tag);
    } else {
      spin2(fl + (F_WQ - L_FL), 1);
      for (int i = 0; i < D1; ++i) __builtin_amdgcn_s_sleep(1);
      u32x2 g0, g1, g2, g3;
      unsigned so2 = 4096;
      for (unsigned it = 0; it < SPIN_MAX; ++it) {
        asm volatile("buffer_load_dwordx2 %0, %4, %5, 0 offen sc1\n\tbuffer_load_dwordx2 %1, %4, %5, 0 offen offset:2048 sc1\n\t"
                     "buffer_load_dwordx2 %2, %4, %5, %6 offen sc1\n\tbuffer_load_dwordx2 %3, %4, %5, %6 offen offset:2048 sc1\n\t"
                     "s_waitcnt vmcnt(0)"
                     : "=&v"(g0), "=&v"(g1), "=&v"(g2), "=&v"(g3) : "v"(o_qg), "s"(rs_ws), "s"(so2) : "memory");
        if (allok(bad(g0.x, g0.y, tag) | bad(g1.x, g1.y, tag) | bad(g2.x, g2.y, tag) | bad(g3.x, g3.y, tag))) break;
      }
      float q = (pay(g0.x) + pay(g1.x)) + (pay(g2.x) + pay(g3.x));
      *lptr<u16>(a_qw) = (u16)pack2(q, q);
      if (l == 0) flag_add(fl + (F_Q - L_FL));
    }
    ST(4);
    spin2(fl + (F_K - L_FL), 4);
    ST(5);
    float sc0, sc1, sc2, sc3;
    {
      bf16x8 k0 = *lptr<bf16x8>(a_k), q0 = *lptr<bf16x8>(a_q), k1 = *lptr<bf16x8>(a_k ^ 64), q1 = *lptr<bf16x8>(a_q + 64);
      bf16x8 k2 = *lptr<bf16x8>(a_k ^ 128), q2 = *lptr<bf16x8>(a_q + 128), k3 = *lptr<bf16x8>(a_k ^ 192), q3 = *lptr<bf16x8>(a_q + 192);
      f32x4 z4 = {0.f, 0.f, 0.f, 0.f};
      f32x4 d = mfma32(k0, q0, z4), e = mfma32(k1, q1, z4);
      d = mfma32(k2, q2, d); e = mfma32(k3, q3, e);
      d += e;
      unsigned p01 = pack2(d[0], d[1]), p23 = pack2(d[2], d[3]);
      p01 = pack2(bflo(p01) * 0.08838834764831845f, bfhi(p01) * 0.08838834764831845f);
      p23 = pack2(bflo(p23) * 0.08838834764831845f, bfhi(p23) * 0.08838834764831845f);
      sc0 = bflo(p01); sc1 = bfhi(p01); sc2 = bflo(p23); sc3 = bfhi(p23);
      float mloc = x32_max(x16_max(fmaxf(fmaxf(sc0, sc1), fmaxf(sc2, sc3))));
      if (l < 2) *lptr<float>(a_mw) = mloc;
    }
    float M;
    {
      u32x4 m0, m1;
      for (unsigned it = 0; it < LSPIN_MAX; ++it) {
        asm volatile("ds_read_b128 %0, %2\n\tds_read_b128 %1, %2 offset:16\n\ts_waitcnt lgkmcnt(0)" : "=&v"(m0), "=&v"(m1) : "v"(a_mr) : "memory");
        unsigned a = max(max(max(m0[0], m0[1]), max(m0[2], m0[3])), max(max(m1[0], m1[1]), max(m1[2], m1[3])));
        if (__builtin_amdgcn_ballot_w64(a == 0xFFFFFFFFu) == 0) break;
      }
      M = fmaxf(fmaxf(fmaxf(__uint_as_float(m0[0]), __uint_as_float(m0[1])), fmaxf(__uint_as_float(m0[2]), __uint_as_float(m0[3]))),
                fmaxf(fmaxf(__uint_as_float(m1[0]), __uint_as_float(m1[1])), fmaxf(__uint_as_float(m1[2]), __uint_as_float(m1[3]))));
      float p0 = fexp(sc0 - M), p1 = fexp(sc1 - M), p2 = fexp(sc2 - M), p3 = fexp(sc3 - M);
      if (n16 < 2) { u32x2 pp = {pack2(p0, p1), pack2(p2, p3)}; *lptr<u32x2>(a_pw) = pp; }
      float lw = x32_sum(x16_sum((p0 + p1) + (p2 + p3)));
      if (l < 2) { *lptr<float>(a_mw + (L_LW - L_MW)) = lw; flag_add(fl + (F_P - L_FL)); }
    }
    ST(6);
    spin2(fl + (F_V - L_FL), 16);
    ST(7);
    {
      f32x4 d = {0.f, 0.f, 0.f, 0.f}, e = {0.f, 0.f, 0.f, 0.f};
      float lwv = *lptr<float>(a_lw);
      bf16x8 v0 = cat(trd(a_v), trd(a_v + 4096)), p0 = *lptr<bf16x8>(a_p), v1 = cat(trd(a_v + 8192), trd(a_v + 8192 + 4096)), p1 = *lptr<bf16x8>(a_p + 64);
      bf16x8 v2 = cat(trd(a_v + 16384), trd(a_v + 16384 + 4096)), p2 = *lptr<bf16x8>(a_p + 128), v3 = cat(trd(a_v + 24576), trd(a_v + 24576 + 4096)), p3 = *lptr<bf16x8>(a_p + 192);
      d = mfma32(v0, p0, d); e = mfma32(v1, p1, e);
      lwv += dpp<0xB1>(lwv); lwv += dpp<0x4E>(lwv); lwv += dpp<0x141>(lwv);
      d = mfma32(v2, p2, d); e = mfma32(v3, p3, e);
      if (w == 0 && (l == 0 || l == 8 || l == 2 || l == 3)) put(ws, o_ml, (l & 7) == 0 ? lwv : M, tag);
      d += e;
      if (n16 < 8) put(ws, o_po, sel4(n16 >> 1, d[0], d[1], d[2], d[3]), tag);
    }
    ST(8);
  }
  const unsigned trb = (8 * kg + q4) * 128 + (((c4 >> 1) ^ trx) * 16) + (c4 & 1) * 8;
  if (orole) {
    const int op = g * 7 + s;
    unsigned o_h3 = WS_H3 + ((((w * 64 + l) >> 3) * 112 + op) * 8 + (l & 7)) * 8;
    unsigned a_wo = LB + L_W + w * 5120 + trb;
    unsigned a_wot = LB + L_W + w * 5120 + 4096 + (4 * (kg & 1) + q4) * 128 + (((c4 >> 1) ^ (2 * ((q4 >> 1) & 1))) * 16) + (c4 & 1) * 8;
    unsigned a_ob = LB + L_OB + kg * 16;
    PIN(o_h3); PIN(a_wo); PIN(a_wot); PIN(a_ob);
    if (w < nu) {
      const int rel = (us + w) * 8 + 2 * kg, head = ((us + w) * 8) >> 7;
      unsigned o0 = WS_PO + ((g * 16 + n16) * 256 + rel) * 8, o1 = WS_ML + ((g * 16 + n16) * 2 + head) * 16;
      unsigned a_ow = LB + L_OB + (w * 8 + 2 * kg) * 2;
      PIN(o0); PIN(o1); PIN(a_ow);
      for (int i = 0; i < D2; ++i) __builtin_amdgcn_s_sleep(1);
      u32x4 a, c;
      for (unsigned it = 0; it < SPIN_MAX; ++it) {
        ld16x2_sc1_wait(a, c, o0, o1, rs_ws);
        if (allok(bad(a.x, a.y, tag) | bad(a.z, a.w, tag) | bad(c.x, c.y, tag) | bad(c.z, c.w, tag))) break;
      }
      float m = pay(c.x), M = row16_max(m), wg = fexp(m - M);
      float L = row16_sum(wg * pay(c.z)), o_0 = row16_sum(wg * pay(a.x)), o_1 = row16_sum(wg * pay(a.z));
      float ri = __builtin_amdgcn_rcpf(L);
      if (n16 == 0) *lptr<unsigned>(a_ow) = pack2(o_0 * ri, o_1 * ri);
    }
    ST(9);
    spin1(LB + F_W);
    bf16x8 bf[4]; s16x4 bg[4];
#pragma unroll
    for (int nb = 0; nb < 4; ++nb) bf[nb] = cat(trd(a_wo ^ (nb * 32)), trd((a_wo ^ (nb * 32)) + 512));
    if (nu == 5) {
#pragma unroll
      for (int nb = 0; nb < 4; ++nb) bg[nb] = trd(a_wot ^ (nb * 32));
    }
    BAR();
    ST(10);
    {
      bf16x8 af = *lptr<bf16x8>(a_ob);
      f32x4 d[4];
#pragma unroll
      for (int nb = 0; nb < 4; ++nb) d[nb] = mfma32(af, bf[nb], (f32x4){0.f, 0.f, 0.f, 0.f});
      if (nu == 5) {
        u32x2 t = *lptr<u32x2>(LB + L_OB + 64 + (kg & 1) * 8);
        if (kg >= 2) t = (u32x2){0u, 0u};
        s16x4 a2 = __builtin_bit_cast(s16x4, t);
#pragma unroll
        for (int nb = 0; nb < 4; ++nb) d[nb] = mfma16(a2, bg[nb], d[nb]);
      }
      put(ws, o_h3, sel4(kg, d[0][0], d[1][0], d[2][0], d[3][0]), tag);
    }
    ST(11);
    if (rd < 72 && w == 0) {
      const unsigned tag32 = (unsigned)epoch;
      unsigned s4 = 4096, s8 = 8192;
      float hv;
      {
        unsigned o0 = WS_H3 + rd * 112 * 64 + l * 8;
        PIN(o0);
        for (int i = 0; i < D3; ++i) __builtin_amdgcn_s_sleep(1);
        u32x2 a[14];
        for (unsigned it = 0; it < SPIN_MAX; ++it) {
#pragma unroll
          for (int i = 0; i < 14; ++i) a[i] = (u32x2){0u, 0u};
#define AOR(n, so, of) "buffer_atomic_or_x2 %" #n ", %[o], %[r], " so " offen offset:" #of " sc0 sc1\n\t"
          asm volatile(AOR(0, "0", 0) AOR(1, "0", 512) AOR(2, "0", 1024) AOR(3, "0", 1536) AOR(4, "0", 2048) AOR(5, "0", 2560) AOR(6, "0", 3072) AOR(7, "0", 3584)
                       AOR(8, "%[s4]", 0) AOR(9, "%[s4]", 512) AOR(10, "%[s4]", 1024) AOR(11, "%[s4]", 1536) AOR(12, "%[s4]", 2048) AOR(13, "%[s4]", 2560)
                       "s_waitcnt vmcnt(0)"
                       : "+v"(a[0]), "+v"(a[1]), "+v"(a[2]), "+v"(a[3]), "+v"(a[4]), "+v"(a[5]), "+v"(a[6]), "+v"(a[7]), "+v"(a[8]), "+v"(a[9]), "+v"(a[10]), "+v"(a[11]), "+v"(a[12]), "+v"(a[13])
                       : [o] "v"(o0), [r] "s"(rs_ws), [s4] "s"(s4) : "memory");
          unsigned b1 = 0, b2 = 0;
#pragma unroll
          for (int i = 0; i < 14; ++i) { b1 |= a[i].y ^ tag.th; b2 |= a[i].x ^ tag.t8; }
          if (allok(b1 | (b2 & 0xFFu))) break;
        }
        float v = (((pay(a[0].x) + pay(a[1].x)) + (pay(a[2].x) + pay(a[3].x))) + ((pay(a[4].x) + pay(a[5].x)) + (pay(a[6].x) + pay(a[7].x)))) +
                  (((pay(a[8].x) + pay(a[9].x)) + (pay(a[10].x) + pay(a[11].x))) + (pay(a[12].x) + pay(a[13].x)));
        v += dpp<0x128>(v);
        v = x32_sum(x16_sum(v));
        hv = rbf(bflo(xr) + rbf(v));
        unsigned hp = pack2(hv, dpp<0x101>(hv));
        unsigned hq = dppu<0x111>(hp);
        if (l & 1) hp = hq;
        if (NREP == 16 || !(l & 1))
          __hip_atomic_store((gu64*)(ws + WS_HB + ((l >> 3) + 8 * (l & 1)) * HB_STRIDE + (rd * 4 + ((l & 7) >> 1)) * 8), ((u64)tag32 << 32) | hp, __ATOMIC_RELAXED, __HIP_MEMORY_SCOPE_AGENT);
      }
      ST(12);
      {
        unsigned o0 = WS_H5 + rd * 144 * 64 + l * 8;
        PIN(o0);
        for (int i = 0; i < D5; ++i) __builtin_amdgcn_s_sleep(1);
        u32x2 a[18];
        for (unsigned it = 0; it < SPIN_MAX; ++it) {
#pragma unroll
          for (int i = 0; i < 18; ++i) a[i] = (u32x2){0u, 0u};
          asm volatile(AOR(0, "0", 0) AOR(1, "0", 512) AOR(2, "0", 1024) AOR(3, "0", 1536) AOR(4, "0", 2048) AOR(5, "0", 2560) AOR(6, "0", 3072) AOR(7, "0", 3584)
                       AOR(8, "%[s4]", 0) AOR(9, "%[s4]", 512) AOR(10, "%[s4]", 1024) AOR(11, "%[s4]", 1536) AOR(12, "%[s4]", 2048) AOR(13, "%[s4]", 2560) AOR(14, "%[s4]", 3072) AOR(15, "%[s4]", 3584)
                       AOR(16, "%[s8]", 0) AOR(17, "%[s8]", 512)
                       "s_waitcnt vmcnt(0)"
                       : "+v"(a[0]), "+v"(a[1]), "+v"(a[2]), "+v"(a[3]), "+v"(a[4]), "+v"(a[5]), "+v"(a[6]), "+v"(a[7]), "+v"(a[8]), "+v"(a[9]), "+v"(a[10]), "+v"(a[11]), "+v"(a[12]), "+v"(a[13]),
                         "+v"(a[14]), "+v"(a[15]), "+v"(a[16]), "+v"(a[17])
                       : [o] "v"(o0), [r] "s"(rs_ws), [s4] "s"(s4), [s8] "s"(s8) : "memory");
          unsigned b1 = 0, b2 = 0;
#pragma unroll
          for (int i = 0; i < 18; ++i) { b1 |= a[i].y ^ tag.th; b2 |= a[i].x ^ tag.t8; }
          if (allok(b1 | (b2 & 0xFFu))) break;
        }
        ST(13);
        float v = (((pay(a[0].x) + pay(a[1].x)) + (pay(a[2].x) + pay(a[3].x))) + ((pay(a[4].x) + pay(a[5].x)) + (pay(a[6].x) + pay(a[7].x)))) +
                  (((pay(a[8].x) + pay(a[9].x)) + (pay(a[10].x) + pay(a[11].x))) + ((pay(a[12].x) + pay(a[13].x)) + (pay(a[14].x) + pay(a[15].x)))) + (pay(a[16].x) + pay(a[17].x));
        v += dpp<0x128>(v);
        v = x32_sum(x16_sum(v));
        float ov = hv + rbf(v);
        unsigned op2 = pack2(ov, dpp<0x101>(ov));
        if (l < 8 && !(l & 1)) *(unsigned*)(out + rd * 8 + l) = op2;
        if (rd == 0 && l == 0) __hip_atomic_store((gu64*)(ws + WS_EPOCH), epoch, __ATOMIC_RELAXED, __HIP_MEMORY_SCOPE_AGENT);
      }
    }
  } else {
    const unsigned tag32 = (unsigned)epoch;
    unsigned o_hb = WS_HB + (REPX ? (NREP == 16 ? xcd * 2 + (f & 1) : xcd) : (f & (NREP - 1))) * HB_STRIDE + (32 * w + 16 * ((n16 >> 1) & 1) + 4 * kg + 2 * (n16 & 1)) * 8;
    unsigned a_w1 = LB + L_W + w * 4096 + (8 * kg + q4) * 64 + (((c4 >> 1) ^ (2 * (kg & 1))) * 16) + (c4 & 1) * 8;
    unsigned a_w2 = LB + L_W2 + w * 4096 + trb;
    unsigned a_ps = LB + L_PART + n16 * 48 + w * 4;
    unsigned a_pt = LB + L_PART + (w < 8 ? 8 * (l & 3) + w : 32 + 8 * (l & 3)) * 48 + 40;
    unsigned a_pr = LB + L_PART + ((n16 < 8 ? 0 : 32) + 8 * kg + (n16 & 7)) * 48;
    unsigned o_h5 = WS_H5 + ((((w * 64 + l) >> 3) * 144 + f) * 8 + (l & 7)) * 8;
    PIN(o_hb); PIN(a_w1); PIN(a_w2); PIN(a_ps); PIN(a_pt); PIN(a_pr); PIN(o_h5);
    spin1(LB + F_W1);
    ST(9);
    bf16x8 b1[2][2], b2[4];
#pragma unroll
    for (int ks = 0; ks < 2; ++ks)
#pragma unroll
      for (int nb = 0; nb < 2; ++nb) b1[ks][nb] = cat(trd((a_w1 ^ (nb * 32)) + ks * 2048), trd((a_w1 ^ (nb * 32)) + ks * 2048 + 256));
    {
      const u32x4 on = {0x3F803F80u, 0x3F803F80u, 0x3F803F80u, 0x3F803F80u};
      const bf16x8 one = __builtin_bit_cast(bf16x8, on);
      f32x4 z4 = {0.f, 0.f, 0.f, 0.f};
      f32x4 c0 = mfma32(one, b1[0][0], z4), c1 = mfma32(one, b1[0][1], z4);
      c0 = mfma32(one, b1[1][0], c0); c1 = mfma32(one, b1[1][1], c1);
      if (l < 16) { *lptr<float>(a_ps + 32 * 48) = c0[0]; *lptr<float>(a_ps + 48 * 48) = c1[0]; }
    }
    u32x4 hg;
    for (int i = 0; i < D4; ++i) __builtin_amdgcn_s_sleep(1);
    for (unsigned it = 0; it < SPIN_MAX; ++it) {
      ld16_sc1_wait(hg, o_hb, rs_ws);
      if (allok((hg.y ^ tag32) | (hg.w ^ tag32))) break;
    }
    ST(10);
    {
      u32x4 A0 = {dppu<0x150>(hg.x), dppu<0x150>(hg.z), dppu<0x151>(hg.x), dppu<0x151>(hg.z)};
      u32x4 A1 = {dppu<0x152>(hg.x), dppu<0x152>(hg.z), dppu<0x153>(hg.x), dppu<0x153>(hg.z)};
      bf16x8 a0 = __builtin_bit_cast(bf16x8, A0), a1 = __builtin_bit_cast(bf16x8, A1);
      f32x4 z4 = {0.f, 0.f, 0.f, 0.f};
      f32x4 d0 = mfma32(a0, b1[0][0], z4), d1 = mfma32(a0, b1[0][1], z4);
      d0 = mfma32(a1, b1[1][0], d0); d1 = mfma32(a1, b1[1][1], d1);
      float s1 = dot2(hg.x, 0x3F803F80u, dot2(hg.z, 0x3F803F80u, 0.f)), s2 = dot2(hg.x, hg.x, dot2(hg.z, hg.z, 0.f));
      s1 += dpp<0xB1>(s1); s2 += dpp<0xB1>(s2); s1 += dpp<0x4E>(s1); s2 += dpp<0x4E>(s2);
      s1 = x32_sum(x16_sum(s1)); s2 = x32_sum(x16_sum(s2));
      if (l < 4) *lptr<f32x2>(a_pt) = (f32x2){s1, s2};
      if (l < 16) { *lptr<float>(a_ps) = d0[0]; *lptr<float>(a_ps + 16 * 48) = d1[0]; }
    }
    spin1(LB + F_W);
#pragma unroll
    for (int nb = 0; nb < 4; ++nb) b2[nb] = cat(trd(a_w2 ^ (nb * 32)), trd((a_w2 ^ (nb * 32)) + 512));
    BAR();
    ST(11);
    {
      f32x4 u0 = *lptr<f32x4>(a_pr), u1 = *lptr<f32x4>(a_pr + 16), u2 = *lptr<f32x4>(a_pr + 32);
      float T = ((u0[0] + u0[1]) + (u0[2] + u0[3])) + ((u1[0] + u1[1]) + (u1[2] + u1[3])) + u2[0];
      float s1 = n16 < 9 ? u2[2] : 0.f, s2 = n16 < 9 ? u2[3] : 0.f;
      s1 = row16_sum(s1); s2 = row16_sum(s2);
      float mean = s1 * (1.f / 576.f), var = fmaxf(s2 * (1.f / 576.f) - mean * mean, 0.f);
      float ri = __builtin_amdgcn_rsqf(var + 1e-5f);
      float C = dpp<0x128>(T);
      float a = rbf((T - mean * C) * ri);
      float act = a * __builtin_amdgcn_rcpf(1.f + fexp(-a));
      unsigned pk = pack2(act, dpp<0x101>(act));
      u32x4 A = {dppu<0x150>(pk), dppu<0x152>(pk), dppu<0x154>(pk), dppu<0x156>(pk)};
      bf16x8 af = __builtin_bit_cast(bf16x8, A);
      f32x4 z4 = {0.f, 0.f, 0.f, 0.f};
      f32x4 d0 = mfma32(af, b2[0], z4), d1 = mfma32(af, b2[1], z4), d2 = mfma32(af, b2[2], z4), d3 = mfma32(af, b2[3], z4);
      put(ws, o_h5, sel4(kg, d0[0], d1[0], d2[0], d3[0]), tag);
    }
    ST(12);
  }
  ST(14);
  STDUMP();
}

extern "C" void kernel_launch(void* const* d_in, const int* in_sizes, int n_in, void* d_out, int out_size, void* d_ws, size_t ws_size,
                              hipStream_t stream) {
  fused_decode_kernel<<<256, NT, 0, stream>>>((const u16*)d_in[0], (const u16*)d_in[1], (const u16*)d_in[2], (const u16*)d_in[3],
                                              (const u16*)d_in[4], (const u16*)d_in[6], (const u16*)d_in[7], (u16*)d_out,
                                              (unsigned char*)d_ws);
}
```

```cpp
#include <hip/hip_runtime.h>
#include <stdint.h>

typedef unsigned short u16;
typedef unsigned long long u64;
typedef unsigned u32x4 __attribute__((ext_vector_type(4)));
typedef unsigned u32x2 __attribute__((ext_vector_type(2)));
typedef float f32x4 __attribute__((ext_vector_type(4)));
typedef float f32x2 __attribute__((ext_vector_type(2)));
typedef short s16x4 __attribute__((ext_vector_type(4)));
typedef short s16x8 __attribute__((ext_vector_type(8)));
typedef __bf16 bf16x8 __attribute__((ext_vector_type(8)));
typedef __bf16 bf16x2 __attribute__((ext_vector_type(2)));
typedef __amdgpu_buffer_rsrc_t rsrc_t;
#define LDS3 __attribute__((address_space(3)))
#define GAS __attribute__((address_space(1)))
typedef GAS u64 gu64;

constexpr int PFC = 24832;
constexpr int PFW = 6;
constexpr int PFX0 = 4, PFX1 = 5;
constexpr int EHW = 5;
constexpr int DQH = 2;
constexpr int HSL = 2;
constexpr int NV0 = 2;
constexpr int DQ = 4;
constexpr int D5 = 20;
constexpr int SBA = 200;
constexpr int SBO = 256;
constexpr int SBT = 96;
constexpr int SBF = 320;
#define NTS " nt"

constexpr int NT = 576;
constexpr unsigned SPIN_MAX = 1u << 18;
constexpr unsigned LSPIN_MAX = SPIN_MAX << 6;
constexpr unsigned WS_EPOCH = 0;
constexpr unsigned HC = 512;
__device__ constexpr unsigned hoff(unsigned s) { return (s & 3) * 128 + (s >> 2) * 4; }
constexpr unsigned WS_QH = 4096;
constexpr unsigned WS_PH = WS_QH + 256 * HC;
constexpr unsigned WS_QS = WS_PH + 256 * HC;
constexpr unsigned WS_PO = WS_QS + 131072;
constexpr unsigned WS_ML = WS_PO + 524288;
constexpr unsigned WS_H3 = WS_ML + 8192;
constexpr unsigned WS_HB = WS_H3 + 516096;
constexpr unsigned HB_STRIDE = 4352;
constexpr unsigned WS_H5 = WS_HB + 16 * HB_STRIDE;
constexpr unsigned WS_END = WS_H5 + 663552;
constexpr unsigned L_WQ = 0;
constexpr unsigned L_PART = 0;
constexpr unsigned L_K = 18432;
constexpr unsigned L_V = L_K + 32768;
constexpr unsigned L_W = L_V + 32768;
constexpr unsigned L_W2 = L_W + 36864;
constexpr unsigned L_S = L_W2 + 36864;
constexpr unsigned L_XN = L_S;
constexpr unsigned L_Q = L_XN + 320;
constexpr unsigned L_P = L_Q + 512;
constexpr unsigned L_FL = L_P + 512;
constexpr unsigned F_WQ = L_FL, F_XN = L_FL + 4, F_K = L_FL + 8, F_Q = L_FL + 12, F_V1 = L_FL + 16, F_P = L_FL + 20, F_V = L_FL + 24, F_W = L_FL + 28;
constexpr unsigned F_W1 = L_FL + 60;
constexpr unsigned F_PO = L_FL + 36, F_QH = L_FL + 40, F_PH = L_FL + 44;
constexpr unsigned F_R3 = L_FL + 48, F_HB = L_FL + 52, F_R5 = L_FL + 56;
constexpr unsigned L_RP = L_WQ;
constexpr unsigned L_MW = L_FL + 64;
constexpr unsigned L_LW = L_MW + 64;
constexpr unsigned L_OB = L_LW + 64;
constexpr unsigned L_PF = L_OB + 128;
constexpr unsigned L_END = L_PF + 1024;
static_assert(L_END <= 163840, "LDS");

__device__ __forceinline__ float bflo(unsigned v) { return __uint_as_float(v << 16); }
__device__ __forceinline__ float bfhi(unsigned v) { return __uint_as_float(v & 0xFFFF0000u); }
__device__ __forceinline__ unsigned pack2(float a, float b) { bf16x2 v = {(__bf16)a, (__bf16)b}; return __builtin_bit_cast(unsigned, v); }
__device__ __forceinline__ float rbf(float f) { return bflo(pack2(f, f)); }
template <int CTRL> __device__ __forceinline__ unsigned dppu(unsigned x) { return (unsigned)__builtin_amdgcn_mov_dpp((int)x, CTRL, 0xf, 0xf, true); }
template <int CTRL> __device__ __forceinline__ float dpp(float x) { return __uint_as_float(dppu<CTRL>(__float_as_uint(x))); }
__device__ __forceinline__ float x16_sum(float x) { auto s = __builtin_amdgcn_permlane16_swap(__float_as_uint(x), __float_as_uint(x), false, false); return __uint_as_float(s[0]) + __uint_as_float(s[1]); }
__device__ __forceinline__ float x32_sum(float x) { auto s = __builtin_amdgcn_permlane32_swap(__float_as_uint(x), __float_as_uint(x), false, false); return __uint_as_float(s[0]) + __uint_as_float(s[1]); }
__device__ __forceinline__ float x16_max(float x) { auto s = __builtin_amdgcn_permlane16_swap(__float_as_uint(x), __float_as_uint(x), false, false); return fmaxf(__uint_as_float(s[0]), __uint_as_float(s[1])); }
__device__ __forceinline__ float x32_max(float x) { auto s = __builtin_amdgcn_permlane32_swap(__float_as_uint(x), __float_as_uint(x), false, false); return fmaxf(__uint_as_float(s[0]), __uint_as_float(s[1])); }
__device__ __forceinline__ float row16_sum(float x) { x += dpp<0xB1>(x); x += dpp<0x4E>(x); x += dpp<0x141>(x); x += dpp<0x140>(x); return x; }
__device__ __forceinline__ float row16_max(float x) { x = fmaxf(x, dpp<0xB1>(x)); x = fmaxf(x, dpp<0x4E>(x)); x = fmaxf(x, dpp<0x141>(x)); x = fmaxf(x, dpp<0x140>(x)); return x; }
__device__ __forceinline__ float dot2(unsigned a, unsigned b, float c) { return __builtin_amdgcn_fdot2_f32_bf16(__builtin_bit_cast(bf16x2, a), __builtin_bit_cast(bf16x2, b), c, false); }
__device__ __forceinline__ float fexp(float x) { return __builtin_amdgcn_exp2f(x * 1.4426950408889634f); }

__device__ __forceinline__ void dma1(unsigned ldsaddr, unsigned voff, rsrc_t r, unsigned soff) {
  asm volatile("s_mov_b32 m0, %0\n\ts_nop 0\n\tbuffer_load_dwordx4 %1, %2, %3 offen" NTS " lds" ::"s"(ldsaddr), "v"(voff), "s"(r), "s"(soff) : "memory");
}
__device__ __forceinline__ void dma4c(unsigned ldsaddr, unsigned v0, unsigned v1, unsigned v2, unsigned v3, rsrc_t r, unsigned soff) {
  asm volatile("s_mov_b32 m0, %0\n\ts_nop 0\n\t"
               "buffer_load_dwordx4 %1, %5, %6 offen" NTS " lds\n\t"
               "buffer_load_dwordx4 %2, %5, %6 offen offset:1024" NTS " lds\n\t"
               "buffer_load_dwordx4 %3, %5, %6 offen offset:2048" NTS " lds\n\t"
               "buffer_load_dwordx4 %4, %5, %6 offen offset:3072" NTS " lds"
               ::"s"(ldsaddr), "v"(v0), "v"(v1), "v"(v2), "v"(v3), "s"(r), "s"(soff) : "memory");
}
#define BAR() asm volatile("s_waitcnt lgkmcnt(0)\n\ts_barrier" ::: "memory")
template <int N> __device__ __forceinline__ void waitvm() { asm volatile("s_waitcnt vmcnt(%0)" ::"n"(N) : "memory"); }
template <int V> struct IC { static constexpr int v = V; };
template <int I, int N, class F> __device__ __forceinline__ void sfor(F&& f) { if constexpr (I < N) { f(IC<I>{}); sfor<I + 1, N>(f); } }
template <bool F> constexpr int uallow(int n, int gs, int sb) {
  int u = 0, t = gs;
  while (u < n && u < 59) { int i = n - 1 - u, c = (F && i >= 82 && i < 118) ? 16 : 8; if (t + c > sb) break; t += c; ++u; }
  return u;
}
constexpr int prevn(int n) { return n <= 18 ? 0 : n - 4; }
__device__ __forceinline__ void hint_spin(const unsigned char* p, unsigned t) {
  _Pragma("unroll 1") for (unsigned it = 0; it < SPIN_MAX; ++it) {
    typedef u64 u64x2 __attribute__((ext_vector_type(2)));
    u64x2 h0, h1, h2, h3;
    asm volatile("s_load_dwordx4 %0, %4, 0x0 glc\n\ts_load_dwordx4 %1, %4, 0x80 glc\n\ts_load_dwordx4 %2, %4, 0x100 glc\n\ts_load_dwordx4 %3, %4, 0x180 glc\n\ts_waitcnt lgkmcnt(0)"
                 : "=&s"(h0), "=&s"(h1), "=&s"(h2), "=&s"(h3) : "s"(p) : "memory");
    const u64 tt = ((u64)t << 32) | t;
    u64 x0 = (h0[0] ^ tt) | (h0[1] ^ tt), x1 = (h1[0] ^ tt) | (h1[1] ^ tt), x2 = (h2[0] ^ tt) | (h2[1] ^ tt), x3 = (h3[0] ^ tt) | (h3[1] ^ tt);
    u64 xx = (x0 | x1) | (x2 | x3);
    asm volatile("" : "+s"(xx));
    if (xx == 0) break;
    for (int i = 0; i < HSL; ++i) __builtin_amdgcn_s_sleep(1);
  }
}
__device__ __forceinline__ unsigned lds_rd(unsigned a) {
  unsigned v;
  asm volatile("ds_read_b32 %0, %1\n\ts_waitcnt lgkmcnt(0)" : "=v"(v) : "v"(a) : "memory");
  return __builtin_amdgcn_readfirstlane(v);
}
#define PIN(x) asm volatile("" : "+v"(x))
#define PINS(x) asm volatile("" : "+s"(x))
__device__ __forceinline__ void ld16_sc1_wait(u32x4& d, unsigned voff, rsrc_t r) {
  asm volatile("buffer_load_dwordx4 %0, %1, %2, 0 offen sc1\n\ts_waitcnt vmcnt(0)" : "=&v"(d) : "v"(voff), "s"(r) : "memory");
}
__device__ __forceinline__ void ld16x2_sc1_wait(u32x4& d0, u32x4& d1, unsigned voff0, unsigned voff1, rsrc_t r) {
  asm volatile("buffer_load_dwordx4 %0, %2, %4, 0 offen sc1\n\tbuffer_load_dwordx4 %1, %3, %4, 0 offen sc1\n\ts_waitcnt vmcnt(0)"
               : "=&v"(d0), "=&v"(d1) : "v"(voff0), "v"(voff1), "s"(r) : "memory");
}
struct Tag { unsigned th, t8; };
__device__ __forceinline__ void put(unsigned char* ws, unsigned off, float v, const Tag& t) {
  unsigned lo = ((__float_as_uint(v) + 0x80u) & 0xFFFFFF00u) | t.t8;
  __hip_atomic_store((gu64*)(ws + off), ((u64)t.th << 32) | lo, __ATOMIC_RELAXED, __HIP_MEMORY_SCOPE_AGENT);
}
__device__ __forceinline__ unsigned bad(unsigned lo, unsigned hi, const Tag& t) { return (hi ^ t.th) | ((lo ^ t.t8) & 0xFFu); }
__device__ __forceinline__ float pay(unsigned lo) { return __uint_as_float(lo & 0xFFFFFF00u); }
__device__ __forceinline__ bool allok(unsigned b) { return __builtin_amdgcn_ballot_w64(b != 0) == 0; }
template <typename T> __device__ __forceinline__ LDS3 T* lptr(unsigned a) { return (LDS3 T*)(uintptr_t)a; }
__device__ __forceinline__ s16x4 trd(unsigned a) { return __builtin_amdgcn_ds_read_tr16_b64_v4i16(lptr<s16x4>(a)); }
__device__ __forceinline__ bf16x8 cat(s16x4 a, s16x4 b) { s16x8 v = __builtin_shufflevector(a, b, 0, 1, 2, 3, 4, 5, 6, 7); return __builtin_bit_cast(bf16x8, v); }
__device__ __forceinline__ f32x4 mfma32(bf16x8 a, bf16x8 b, f32x4 c) { return __builtin_amdgcn_mfma_f32_16x16x32_bf16(a, b, c, 0, 0, 0); }
__device__ __forceinline__ f32x4 mfma16(s16x4 a, s16x4 b, f32x4 c) { return __builtin_amdgcn_mfma_f32_16x16x16bf16_1k(a, b, c, 0, 0, 0); }
__device__ __forceinline__ float sel4(int rg, float a, float b, float c, float d) { float x = rg & 1 ? b : a, y = rg & 1 ? d : c; return rg & 2 ? y : x; }
__device__ __forceinline__ void flag_set(unsigned a) { unsigned one = 1; asm volatile("ds_write_b32 %0, %1" ::"v"(a), "v"(one) : "memory"); }
__device__ __forceinline__ void flag_add(unsigned a) { unsigned one = 1; asm volatile("ds_add_u32 %0, %1" ::"v"(a), "v"(one) : "memory"); }
__device__ __forceinline__ void spin1(unsigned a) {
  _Pragma("unroll 1") for (unsigned it = 0; it < LSPIN_MAX; ++it) {
    unsigned v;
    asm volatile("ds_read_b32 %0, %1\n\ts_waitcnt lgkmcnt(0)" : "=v"(v) : "v"(a) : "memory");
    if (__builtin_amdgcn_readfirstlane(v) != 0) break;
  }
}
__device__ __forceinline__ void spin_eq(unsigned a, unsigned n) {
  _Pragma("unroll 1") for (unsigned it = 0; it < LSPIN_MAX; ++it) {
    unsigned v;
    asm volatile("ds_read_b32 %0, %1\n\ts_waitcnt lgkmcnt(0)" : "=v"(v) : "v"(a) : "memory");
    if (__builtin_amdgcn_readfirstlane(v) == n) break;
  }
}
__device__ __forceinline__ void spin2(unsigned a, unsigned n1) {
  _Pragma("unroll 1") for (unsigned it = 0; it < LSPIN_MAX; ++it) {
    u32x2 v;
    asm volatile("ds_read_b64 %0, %1\n\ts_waitcnt lgkmcnt(0)" : "=v"(v) : "v"(a) : "memory");
    if (__builtin_amdgcn_readfirstlane(v.x == 0 ? 0u : v.y) == n1) break;
  }
}
__device__ __forceinline__ void poll16(unsigned o, unsigned a, rsrc_t r, const Tag& t, int l) {
  unsigned vo = o + l * 8;
  u32x2 a0, a1;
  _Pragma("unroll 1") for (unsigned it = 0; it < SPIN_MAX; ++it) {
    a0 = (u32x2){0u, 0u}; a1 = (u32x2){0u, 0u};
    asm volatile("buffer_atomic_or_x2 %0, %2, %3, 0 offen sc0 sc1\n\tbuffer_atomic_or_x2 %1, %2, %3, 0 offen offset:512 sc0 sc1\n\ts_waitcnt vmcnt(0)"
                 : "+v"(a0), "+v"(a1) : "v"(vo), "s"(r) : "memory");
    if (allok(bad(a0.x, a0.y, t) | bad(a1.x, a1.y, t))) break;
  }
  float v = pay(a0.x) + pay(a1.x);
  v += dpp<0x128>(v);
  v = x32_sum(x16_sum(v));
  if (l < 8) *lptr<float>(a + l * 4) = v;
}

__global__ void __launch_bounds__(NT, 3) __attribute__((amdgpu_num_vgpr(96)))
fused_decode_kernel(const u16* __restrict__ x, const u16* __restrict__ kc, const u16* __restrict__ vc, const u16* __restrict__ wq,
                    const u16* __restrict__ wo, const u16* __restrict__ w1, const u16* __restrict__ w2, u16* __restrict__ out,
                    unsigned char* __restrict__ ws) {
  __shared__ __attribute__((aligned(1024))) unsigned char lds[L_END];
  unsigned long long pc0;
  asm volatile("s_getpc_b64 %0" : "=s"(pc0));
  const int tid = threadIdx.x, l = tid & 63;
  int w = __builtin_amdgcn_readfirstlane(tid >> 6);
  asm volatile("s_nop 4" : "+s"(w));
  const int b = blockIdx.x, xcd = b & 7, r = b >> 3, g = xcd * 2 + (r >> 4), s = r & 15;
  const unsigned LB = (unsigned)(uintptr_t)(LDS3 unsigned char*)lds;
  const int n16 = l & 15, kg = l >> 4, q4 = (l >> 2) & 3, c4 = l & 3;
  const int kq = s >> 2, cb = s & 3;
  const int rd = s * 16 + g;
  const unsigned sw0 = ((l & 7) ^ (2 * ((l >> 4) & 1))) * 16;
  unsigned xr = 0, xr0;
  u32x4 xa, xb, xq;
  {
    if (w == PFW) {
      const rsrc_t rs_c = __builtin_amdgcn_make_buffer_rsrc((void*)(pc0 & ~255ull), 0, PFC, 0x00020000);
      unsigned oc = r * 1024 + l * 16;
      asm volatile("s_mov_b32 m0, %0\n\ts_nop 0\n\tbuffer_load_dwordx4 %1, %2, 0 offen lds" ::"s"(LB + L_PF), "v"(oc), "s"(rs_c) : "memory");
    }
    unsigned vq0 = (l >> 3) * 8192 + sw0, vq1 = vq0 ^ 64;
    unsigned so = (kq * 144) * 8192 + (g * 256 + cb * 64) * 2 + w * 131072, m0q = LB + L_WQ + w * 2048;
    unsigned o16 = l * 16, oq = kq * 288 + l * 16, ox = w == 0 ? (rd * 8 + (l & 7)) * 2 : 0x7FFFFFFFu;
    PIN(vq0); PIN(vq1); PIN(o16); PIN(oq); PIN(ox); PINS(so); PINS(m0q);
    asm volatile("" ::"s"(x), "s"(kc), "s"(vc), "s"(wq), "s"(wo), "s"(w1), "s"(w2), "s"(out), "s"(ws));
    const rsrc_t rs_wq = __builtin_amdgcn_make_buffer_rsrc((void*)wq, 0, 576 * 8192, 0x00020000);
    const rsrc_t rs_x = __builtin_amdgcn_make_buffer_rsrc((void*)x, 0, 1152, 0x00020000);
    if (w == 7)
      asm volatile("buffer_load_dwordx4 %0, %3, %5, 0 offen\n\t"
                   "buffer_load_dwordx4 %1, %3, %5, 0 offen offset:1024\n\t"
                   "buffer_load_dwordx4 %2, %4, %5, 0 offen"
                   : "=&v"(xa), "=&v"(xb), "=&v"(xq) : "v"(o16), "v"(oq), "s"(rs_x) : "memory");
    if (w != PFW) {
      dma1(m0q, vq0, rs_wq, so);
      dma1(m0q + 1024, vq1, rs_wq, so + 65536);
      if (w == PFX0) dma1(LB + L_WQ + PFW * 2048, vq0, rs_wq, so + (PFW - PFX0) * 131072);
      if (w == PFX1) dma1(LB + L_WQ + PFW * 2048 + 1024, vq1, rs_wq, so + (PFW - PFX1) * 131072 + 65536);
    }
    if (w == PFW) xr0 = 0;
    else asm volatile("buffer_load_ushort %0, %1, %2, 0 offen" : "=&v"(xr0) : "v"(ox), "s"(rs_x) : "memory");
  }
  if (w == 0 && l < 32) *(volatile LDS3 unsigned*)(uintptr_t)(LB + L_FL + l * 4) = l < 16 ? 0u : 0xFFFFFFFFu;
  BAR();
#define LOAD_EPOCH() (*(volatile const __attribute__((address_space(4))) u64*)(uintptr_t)ws)
  const rsrc_t rs_ws = __builtin_amdgcn_make_buffer_rsrc((void*)ws, 0, WS_END, 0x00020000);
  const bool orole = s < 7;
  const int us = s < 4 ? 5 * s : 20 + 4 * (s - 4), nu = s < 4 ? 5 : 4;
  const int f = g * 9 + (s - 7);
  Tag tag;
  u64 epoch;
  const unsigned trx = (2 * (((q4 >> 1) & 1) | ((kg & 1) << 1)));

  if (__builtin_expect(w == 8, 1)) {
    __builtin_amdgcn_s_setprio(3);
    const size_t kvoff = ((size_t)g * 8192 + 6144 + s * 16) * 128;
    const unsigned kvst = 65536, kvsz = 7 * 65536 + 4096;
    const rsrc_t rs_k = __builtin_amdgcn_make_buffer_rsrc((void*)(kc + kvoff), 0, kvsz, 0x00020000);
    const rsrc_t rs_v = __builtin_amdgcn_make_buffer_rsrc((void*)(vc + kvoff), 0, kvsz, 0x00020000);
    const u64 ep = LOAD_EPOCH();
    unsigned fl = LB + L_FL;
    PIN(fl);
#define GATE(F, n, gs, SB) GATE2(F, n, gs, SB, (SBF > 256 ? SBF : 256))
#define GATE2(F, n, gs, SB, SBM) do { \
      constexpr int u_ = uallow<F>(n, gs, SB), pn_ = prevn(n), pu_ = uallow<F>(pn_, 0, SBM); \
      constexpr int t1_ = F ? 118 : 1000; \
      if constexpr ((n) - u_ >= 18 && ((n) == 18 || pn_ - pu_ < 18)) { waitvm<((n) - 18 > 63 ? 63 : (n) - 18)>(); if (l == 0) flag_set(fl + (F_WQ - L_FL)); } \
      if constexpr ((n) - u_ >= 50 && pn_ - pu_ < 50) { waitvm<((n) - 50 > 63 ? 63 : (n) - 50)>(); if (l == 0) flag_set(fl + (F_K - L_FL)); } \
      if constexpr ((n) - u_ >= 74 && pn_ - pu_ < 74) { waitvm<((n) - 74 > 63 ? 63 : (n) - 74)>(); if (l == 0) flag_set(fl + (F_V1 - L_FL)); } \
      if constexpr ((n) - u_ >= 82 && pn_ - pu_ < 82) { waitvm<((n) - 82 > 63 ? 63 : (n) - 82)>(); if (l == 0) flag_set(fl + (F_V - L_FL)); } \
      if constexpr ((n) - u_ >= t1_ && pn_ - pu_ < t1_) { waitvm<((n) - t1_ > 63 ? 63 : ((n) - t1_ < 0 ? 0 : (n) - t1_))>(); if (l == 0) flag_set(fl + (F_W1 - L_FL)); } \
      if constexpr (u_ < (n)) waitvm<u_>(); } while (0)
    {
      const unsigned vk0 = kg * 256 + ((n16 ^ kg) * 16);
      sfor<0, 8>([&](auto T) { constexpr int t = T.v; GATE(false, 18 + 4 * t, 32, SBA); dma4c(LB + L_K + t * 4096, vk0, vk0 ^ 64, vk0 ^ 128, vk0 ^ 192, rs_k, t * kvst); });
    }
    {
      const unsigned vv0 = kg * 256 + ((n16 ^ (2 * kg)) * 16), vv1 = vv0 ^ 128;
      sfor<0, 8>([&](auto T) { constexpr int t = T.v;
        if constexpr (t == NV0) {
          waitvm<32 + 4 * t>(); if (l == 0) flag_set(fl + (F_WQ - L_FL));
          waitvm<4 * t>(); if (l == 0) flag_set(fl + (F_K - L_FL));
          spin1(fl + (F_QH - L_FL));
          for (int i = 0; i < DQ; ++i) __builtin_amdgcn_s_sleep(1);
        }
        GATE(false, 50 + 4 * t, 32, SBA); dma4c(LB + L_V + t * 4096, vv0, vv1, vv0, vv1, rs_v, t * kvst); });
    }
    const unsigned vw0 = (l >> 3) * 1152 + sw0, vw1 = vw0 ^ 64;
#define WTILE(rs, wl, wst, wso, t) do { const unsigned m_ = (wl) + (t) * (wst), s_ = (wso) + (t) * 128; \
      dma1(m_, vw0, rs, s_); dma1(m_ + 1024, vw1, rs, s_ + 9216); dma1(m_ + 2048, vw0, rs, s_ + 18432); dma1(m_ + 3072, vw1, rs, s_ + 27648); } while (0)
    if (orole) {
      const rsrc_t rs_o = __builtin_amdgcn_make_buffer_rsrc((void*)wo, 0, 4096 * 1152, 0x00020000);
      const unsigned wso = (g * 256 + us * 8) * 1152;
      sfor<0, 9>([&](auto T) { constexpr int t = T.v;
        GATE(false, 82 + 4 * t, 32, SBO);
        WTILE(rs_o, LB + L_W, 5120, wso, t); });
      if (nu == 5) {
        waitvm<uallow<false>(118, 72, SBO)>();
#pragma unroll
        for (int t = 0; t < 9; ++t) dma1(LB + L_W + t * 5120 + 4096, vw0, rs_o, wso + t * 128 + 36864);
      }
    } else {
      const rsrc_t rs_w1 = __builtin_amdgcn_make_buffer_rsrc((void*)w1, 0, 576 * 9216, 0x00020000);
      const rsrc_t rs_w2 = __builtin_amdgcn_make_buffer_rsrc((void*)w2, 0, 4608 * 1152, 0x00020000);
      const unsigned v1 = (l >> 2) * 9216 + (((l & 3) ^ (2 * ((l >> 5) & 1))) * 16);
      bool full = false;
#define FGATE(n, gs) do { if (!full) full = lds_rd(fl + (F_PO - L_FL)) == 8; if (full) GATE(true, n, gs, SBF); else GATE(true, n, gs, SBT); } while (0)
      sfor<0, 9>([&](auto T) { constexpr int t = T.v; FGATE(82 + 4 * t, 64);
        const unsigned m_ = LB + L_W + t * 4096, s_ = (t * 64) * 9216 + f * 64;
        dma1(m_, v1, rs_w1, s_); dma1(m_ + 1024, v1, rs_w1, s_ + 16 * 9216); dma1(m_ + 2048, v1, rs_w1, s_ + 32 * 9216); dma1(m_ + 3072, v1, rs_w1, s_ + 48 * 9216); });
      const unsigned wso = f * 32 * 1152;
      sfor<0, 9>([&](auto T) { constexpr int t = T.v; FGATE(118 + 4 * t, 32); WTILE(rs_w2, LB + L_W2, 4096, wso, t); });
    }
    waitvm<0>();
    if (l == 0) { flag_set(fl + (F_WQ - L_FL)); flag_set(fl + (F_K - L_FL)); flag_set(fl + (F_V1 - L_FL)); flag_set(fl + (F_V - L_FL)); flag_set(fl + (F_W1 - L_FL)); flag_set(fl + (F_W - L_FL)); }
    __builtin_amdgcn_s_setprio(0);
    epoch = ep + 1; tag.th = (unsigned)(epoch >> 8); tag.t8 = (unsigned)epoch & 0xFFu;
  } else {
    const u64 ep = LOAD_EPOCH();
    unsigned a_wq = LB + L_WQ + (8 * kg + q4) * 128 + (((2 * (w & 3) + (c4 >> 1)) ^ trx) * 16) + (c4 & 1) * 8;
    unsigned a_wqt = LB + L_WQ + (128 + 8 * (kg & 1) + 4 * (kg >> 1) + q4) * 128 + (((2 * (w & 3) + (c4 >> 1)) ^ trx) * 16) + (c4 & 1) * 8;
    unsigned a_xn = LB + L_XN + kg * 16, a_xnt = LB + L_XN + 256 + 16 * (kg & 1) + 8 * (kg >> 1);
    unsigned o_qp = WS_QS + ((g * 4 + kq) * 256 + cb * 64 + (w & 3) * 16 + n16) * 8;
    unsigned o_qg = WS_QS + (g * 4 * 256 + (w & 3) * 64 + l) * 8;
    unsigned a_qw = LB + L_Q + ((w & 3) * 64 + l) * 2;
    unsigned a_k = LB + L_K + w * 4096 + n16 * 256 + ((kg ^ n16) * 16);
    unsigned a_q = LB + L_Q + (n16 & 1) * 256 + kg * 16;
    unsigned a_mw = LB + L_MW + ((l & 1) * 8 + w) * 4;
    unsigned a_mr = LB + L_MW + (n16 & 1) * 32;
    unsigned a_pw = LB + L_P + (n16 & 1) * 256 + (32 * (w >> 1) + 8 * kg + 4 * (w & 1)) * 2;
    unsigned a_v = LB + L_V + (4 * kg + q4) * 256 + (((2 * w + (c4 >> 1)) ^ (2 * ((4 * kg + q4) & 7))) * 16) + (c4 & 1) * 8;
    unsigned a_p = LB + L_P + (n16 & 1) * 256 + kg * 16;
    unsigned o_po = WS_PO + ((g * 16 + s) * 256 + (n16 & 1) * 128 + 16 * w + 4 * kg + (n16 >> 1)) * 8;
    unsigned a_lw = LB + L_LW + n16 * 4;
    unsigned o_ml = WS_ML + (((g * 16 + s) * 2 + ((l & 7) == 0 ? (l >> 3) : (l & 1))) * 2 + ((l & 7) == 0 ? 1 : 0)) * 8;
    unsigned fl = LB + L_FL;
    PIN(a_wq); PIN(a_wqt); PIN(a_xn); PIN(a_xnt); PIN(o_qp); PIN(o_qg); PIN(a_qw); PIN(a_k); PIN(a_q); PIN(a_mw); PIN(a_mr); PIN(a_pw); PIN(a_v); PIN(a_p); PIN(o_po);
    PIN(a_lw); PIN(o_ml); PIN(fl);
    if (w == 7) {
      asm volatile("s_waitcnt vmcnt(%3)" : "+v"(xa), "+v"(xb), "+v"(xq) : "n"(3) : "memory");
      float s1 = 0.f, s2 = 0.f, t1 = 0.f, t2 = 0.f;
#pragma unroll
      for (int i = 0; i < 4; ++i) { s1 = dot2(xa[i], 0x3F803F80u, s1); s2 = dot2(xa[i], xa[i], s2); t1 = dot2(xb[i], 0x3F803F80u, t1); t2 = dot2(xb[i], xb[i], t2); }
      s1 += t1; s2 += t2;
      s1 = x32_sum(x16_sum(row16_sum(s1))); s2 = x32_sum(x16_sum(row16_sum(s2)));
      float mean = s1 * (1.f / 576.f), var = fmaxf(s2 * (1.f / 576.f) - mean * mean, 0.f);
      float mb = rbf(mean), ri = __builtin_amdgcn_rcpf(rbf(__builtin_amdgcn_sqrtf(rbf(rbf(var) + 1e-5f))));
      u32x4 a;
#pragma unroll
      for (int i = 0; i < 4; ++i) a[i] = pack2(rbf(bflo(xq[i]) - mb) * ri, rbf(bfhi(xq[i]) - mb) * ri);
      if (l < 18) *lptr<u32x4>(LB + L_XN + l * 16) = a;
      waitvm<0>();
      if (l == 0) flag_add(fl + (F_XN - L_FL));
    }
    if (w < 7 && w != PFW) { asm volatile("s_waitcnt vmcnt(%2)\n\tv_mov_b32 %0, %1" : "=v"(xr) : "v"(xr0), "n"(0) : "memory"); if (l == 0) flag_add(fl + (F_XN - L_FL)); }
    epoch = ep + 1; tag.th = (unsigned)(epoch >> 8); tag.t8 = (unsigned)epoch & 0xFFu;
    PINS(tag.th); PINS(tag.t8);
    if (w < 4) {
      spin2(fl + (F_WQ - L_FL), 7);
      f32x4 z4 = {0.f, 0.f, 0.f, 0.f};
      bf16x8 a0 = *lptr<bf16x8>(a_xn), a1 = *lptr<bf16x8>(a_xn + 64), a2 = *lptr<bf16x8>(a_xn + 128), a3 = *lptr<bf16x8>(a_xn + 192);
      s16x4 at = *lptr<s16x4>(a_xnt);
      bf16x8 b0 = cat(trd(a_wq), trd(a_wq + 512)), b1 = cat(trd(a_wq + 4096), trd(a_wq + 4096 + 512));
      bf16x8 b2 = cat(trd(a_wq + 8192), trd(a_wq + 8192 + 512)), b3 = cat(trd(a_wq + 12288), trd(a_wq + 12288 + 512));
      s16x4 bt = trd(a_wqt);
      f32x4 d = mfma32(a0, b0, z4), e = mfma32(a1, b1, z4);
      d = mfma32(a2, b2, d); e = mfma32(a3, b3, e);
      d = mfma16(at, bt, d);
      if (l < 16) put(ws, o_qp, d[0] + e[0], tag);
    } else {
      spin2(fl + (F_WQ - L_FL), 7);
      if (w == EHW) {
        if (l < 16) __hip_atomic_store((GAS unsigned*)(ws + WS_QH + (g * 16 + l) * HC + hoff(s)), (unsigned)epoch, __ATOMIC_RELAXED, __HIP_MEMORY_SCOPE_AGENT);
      }
      if (w == 4) for (int i = 0; i < DQH; ++i) __builtin_amdgcn_s_sleep(1);
      if (w == 4) hint_spin(ws + WS_QH + (g * 16 + s) * HC, (unsigned)epoch);
      if (w == 4) { if (l == 0) flag_set(fl + (F_QH - L_FL)); } else spin1(fl + (F_QH - L_FL));
      u32x2 g0, g1, g2, g3;
      unsigned so2 = 4096;
      _Pragma("unroll 1") for (unsigned it = 0; it < SPIN_MAX; ++it) {
        asm volatile("buffer_load_dwordx2 %0, %4, %5, 0 offen sc1\n\tbuffer_load_dwordx2 %1, %4, %5, 0 offen offset:2048 sc1\n\t"
                     "buffer_load_dwordx2 %2, %4, %5, %6 offen sc1\n\tbuffer_load_dwordx2 %3, %4, %5, %6 offen offset:2048 sc1\n\t"
                     "s_waitcnt vmcnt(0)"
                     : "=&v"(g0), "=&v"(g1), "=&v"(g2), "=&v"(g3) : "v"(o_qg), "s"(rs_ws), "s"(so2) : "memory");
        if (allok(bad(g0.x, g0.y, tag) | bad(g1.x, g1.y, tag) | bad(g2.x, g2.y, tag) | bad(g3.x, g3.y, tag))) break;
      }
      float q = (pay(g0.x) + pay(g1.x)) + (pay(g2.x) + pay(g3.x));
      *lptr<u16>(a_qw) = (u16)pack2(q, q);
      if (l == 0) flag_add(fl + (F_Q - L_FL));
    }
    spin2(fl + (F_K - L_FL), 4);
    float sc0, sc1, sc2, sc3;
    {
      bf16x8 k0 = *lptr<bf16x8>(a_k), q0 = *lptr<bf16x8>(a_q), k1 = *lptr<bf16x8>(a_k ^ 64), q1 = *lptr<bf16x8>(a_q + 64);
      bf16x8 k2 = *lptr<bf16x8>(a_k ^ 128), q2 = *lptr<bf16x8>(a_q + 128), k3 = *lptr<bf16x8>(a_k ^ 192), q3 = *lptr<bf16x8>(a_q + 192);
      f32x4 z4 = {0.f, 0.f, 0.f, 0.f};
      f32x4 d = mfma32(k0, q0, z4), e = mfma32(k1, q1, z4);
      d = mfma32(k2, q2, d); e = mfma32(k3, q3, e);
      d += e;
      unsigned p01 = pack2(d[0], d[1]), p23 = pack2(d[2], d[3]);
      p01 = pack2(bflo(p01) * 0.08838834764831845f, bfhi(p01) * 0.08838834764831845f);
      p23 = pack2(bflo(p23) * 0.08838834764831845f, bfhi(p23) * 0.08838834764831845f);
      sc0 = bflo(p01); sc1 = bfhi(p01); sc2 = bflo(p23); sc3 = bfhi(p23);
      float mloc = x32_max(x16_max(fmaxf(fmaxf(sc0, sc1), fmaxf(sc2, sc3))));
      if (l < 2) *lptr<float>(a_mw) = mloc;
    }
    float M;
    {
      u32x4 m0, m1;
      _Pragma("unroll 1") for (unsigned it = 0; it < LSPIN_MAX; ++it) {
        asm volatile("ds_read_b128 %0, %2\n\tds_read_b128 %1, %2 offset:16\n\ts_waitcnt lgkmcnt(0)" : "=&v"(m0), "=&v"(m1) : "v"(a_mr) : "memory");
        unsigned a = max(max(max(m0[0], m0[1]), max(m0[2], m0[3])), max(max(m1[0], m1[1]), max(m1[2], m1[3])));
        if (__builtin_amdgcn_ballot_w64(a == 0xFFFFFFFFu) == 0) break;
      }
      M = fmaxf(fmaxf(fmaxf(__uint_as_float(m0[0]), __uint_as_float(m0[1])), fmaxf(__uint_as_float(m0[2]), __uint_as_float(m0[3]))),
                fmaxf(fmaxf(__uint_as_float(m1[0]), __uint_as_float(m1[1])), fmaxf(__uint_as_float(m1[2]), __uint_as_float(m1[3]))));
      float p0 = fexp(sc0 - M), p1 = fexp(sc1 - M), p2 = fexp(sc2 - M), p3 = fexp(sc3 - M);
      if (n16 < 2) { u32x2 pp = {pack2(p0, p1), pack2(p2, p3)}; *lptr<u32x2>(a_pw) = pp; }
      float lw = x32_sum(x16_sum((p0 + p1) + (p2 + p3)));
      if (l < 2) { *lptr<float>(a_mw + (L_LW - L_MW)) = lw; flag_add(fl + (F_P - L_FL)); }
    }
    bool vall;
    _Pragma("unroll 1") for (unsigned it = 0; it < LSPIN_MAX; ++it) {
      u32x4 v;
      asm volatile("ds_read_b128 %0, %1 offset:16\n\ts_waitcnt lgkmcnt(0)" : "=v"(v) : "v"(fl) : "memory");
      vall = __builtin_amdgcn_readfirstlane(v.z) != 0;
      if (__builtin_amdgcn_readfirstlane(v.x == 0 ? 0u : v.y) == 16) break;
    }
    if (w == 7 && l < 7)
      __hip_atomic_store((GAS unsigned*)(ws + WS_PH + (g * 16 + l) * HC + hoff(s)), (unsigned)epoch, __ATOMIC_RELAXED, __HIP_MEMORY_SCOPE_AGENT);
    {
      f32x4 d = {0.f, 0.f, 0.f, 0.f}, e = {0.f, 0.f, 0.f, 0.f};
      float lwv = *lptr<float>(a_lw);
      bf16x8 v0 = cat(trd(a_v), trd(a_v + 4096)), p0 = *lptr<bf16x8>(a_p), v1 = cat(trd(a_v + 8192), trd(a_v + 8192 + 4096)), p1 = *lptr<bf16x8>(a_p + 64);
      bf16x8 v2 = cat(trd(a_v + 16384), trd(a_v + 16384 + 4096)), p2 = *lptr<bf16x8>(a_p + 128), p3 = *lptr<bf16x8>(a_p + 192);
      d = mfma32(v0, p0, d); e = mfma32(v1, p1, e);
      lwv += dpp<0xB1>(lwv); lwv += dpp<0x4E>(lwv); lwv += dpp<0x141>(lwv);
      d = mfma32(v2, p2, d);
      if (w == 0 && (l == 0 || l == 8 || l == 2 || l == 3)) put(ws, o_ml, (l & 7) == 0 ? lwv : M, tag);
      asm volatile("" : "+v"(d), "+v"(e), "+v"(p3));
      if (!vall) spin1(fl + (F_V - L_FL));
      bf16x8 v3 = cat(trd(a_v + 24576), trd(a_v + 24576 + 4096));
      e = mfma32(v3, p3, e);
      d += e;
      if (n16 < 8) put(ws, o_po, sel4(n16 >> 1, d[0], d[1], d[2], d[3]), tag);
      if (l == 0) flag_add(fl + (F_PO - L_FL));
    }
  }
  const unsigned trb = (8 * kg + q4) * 128 + (((c4 >> 1) ^ trx) * 16) + (c4 & 1) * 8;
  if (orole) {
    const int op = g * 7 + s;
    unsigned o_h3 = WS_H3 + ((((w * 64 + l) >> 3) * 112 + op) * 8 + (l & 7)) * 8;
    unsigned a_wo = LB + L_W + w * 5120 + trb;
    unsigned a_wot = LB + L_W + w * 5120 + 4096 + (4 * (kg & 1) + q4) * 128 + (((c4 >> 1) ^ (2 * ((q4 >> 1) & 1))) * 16) + (c4 & 1) * 8;
    unsigned a_ob = LB + L_OB + kg * 16;
    PIN(o_h3); PIN(a_wo); PIN(a_wot); PIN(a_ob);
    bf16x8 bf[4]; s16x4 bg[4];
    auto rdw = [&]() {
#pragma unroll
      for (int nb = 0; nb < 4; ++nb) bf[nb] = cat(trd(a_wo ^ (nb * 32)), trd((a_wo ^ (nb * 32)) + 512));
      if (nu == 5) {
#pragma unroll
        for (int nb = 0; nb < 4; ++nb) bg[nb] = trd(a_wot ^ (nb * 32));
      }
    };
    {
      const int rel = (us + w) * 8 + 2 * kg, head = ((us + w) * 8) >> 7;
      unsigned o0 = WS_PO + ((g * 16 + n16) * 256 + rel) * 8, o1 = WS_ML + ((g * 16 + n16) * 2 + head) * 16;
      unsigned a_ow = LB + L_OB + (w * 8 + 2 * kg) * 2;
      PIN(o0); PIN(o1); PIN(a_ow);
      u32x4 a, c;
      if (w < nu) {
        if (w == 0) hint_spin(ws + WS_PH + (g * 16 + s) * HC, (unsigned)epoch);
        if (w == 0) { if (l == 0) flag_set(LB + F_PH); } else spin1(LB + F_PH);
        asm volatile("buffer_load_dwordx4 %0, %2, %4, 0 offen sc1\n\tbuffer_load_dwordx4 %1, %3, %4, 0 offen sc1"
                     : "=&v"(a), "=&v"(c) : "v"(o0), "v"(o1), "s"(rs_ws) : "memory");
      } else spin1(LB + F_W);
      const bool pre = lds_rd(LB + F_W) != 0;
      if (pre) rdw();
      if (w < nu) {
        asm volatile("s_waitcnt vmcnt(0)" : "+v"(a), "+v"(c)::"memory");
        _Pragma("unroll 1") for (unsigned it = 0; it < SPIN_MAX; ++it) {
          if (allok(bad(a.x, a.y, tag) | bad(a.z, a.w, tag) | bad(c.x, c.y, tag) | bad(c.z, c.w, tag))) break;
          ld16x2_sc1_wait(a, c, o0, o1, rs_ws);
        }
        float m = pay(c.x), M = row16_max(m), wg = fexp(m - M);
        float L = row16_sum(wg * pay(c.z)), o_0 = row16_sum(wg * pay(a.x)), o_1 = row16_sum(wg * pay(a.z));
        float ri = __builtin_amdgcn_rcpf(L);
        if (n16 == 0) *lptr<unsigned>(a_ow) = pack2(o_0 * ri, o_1 * ri);
      }
      if (!pre) { spin1(LB + F_W); rdw(); }
    }
    BAR();
    {
      bf16x8 af = *lptr<bf16x8>(a_ob);
      f32x4 d[4];
#pragma unroll
      for (int nb = 0; nb < 4; ++nb) d[nb] = mfma32(af, bf[nb], (f32x4){0.f, 0.f, 0.f, 0.f});
      if (nu == 5) {
        u32x2 t = *lptr<u32x2>(LB + L_OB + 64 + (kg & 1) * 8);
        if (kg >= 2) t = (u32x2){0u, 0u};
        s16x4 a2 = __builtin_bit_cast(s16x4, t);
#pragma unroll
        for (int nb = 0; nb < 4; ++nb) d[nb] = mfma16(a2, bg[nb], d[nb]);
      }
      put(ws, o_h3, sel4(kg, d[0][0], d[1][0], d[2][0], d[3][0]), tag);
    }
    if (rd < 72) {
      const unsigned tag32 = (unsigned)epoch;
      float hv = 0.f;
      unsigned flr = LB + L_FL, a_rp = LB + L_RP + l * 4;
      PIN(flr); PIN(a_rp);
      if (w < 7) {
        poll16(WS_H3 + rd * 112 * 64 + w * 1024, LB + L_RP + w * 32, rs_ws, tag, l);
        if (l == 0) flag_add(flr + (F_R3 - L_FL));
      }
      if (w == 0) {
        spin_eq(flr + (F_R3 - L_FL), 7);
        float v = *lptr<float>(a_rp);
        if (l >= 56) v = 0.f;
        v += dpp<0x128>(v);
        v = x32_sum(x16_sum(v));
        hv = rbf(bflo(xr) + rbf(v));
        unsigned hp = pack2(hv, dpp<0x101>(hv));
        unsigned hq = dppu<0x111>(hp);
        if (l & 1) hp = hq;
        __hip_atomic_store((gu64*)(ws + WS_HB + ((l >> 3) + 8 * (l & 1)) * HB_STRIDE + (rd * 4 + ((l & 7) >> 1)) * 8), ((u64)tag32 << 32) | hp, __ATOMIC_RELAXED, __HIP_MEMORY_SCOPE_AGENT);
        if (l == 0) flag_set(flr + (F_HB - L_FL));
      } else spin1(flr + (F_HB - L_FL));
      for (int i = 0; i < D5; ++i) __builtin_amdgcn_s_sleep(1);
      poll16(WS_H5 + rd * 144 * 64 + w * 1024, LB + L_RP + 512 + w * 32, rs_ws, tag, l);
      if (l == 0) flag_add(flr + (F_R5 - L_FL));
      if (w == 0) {
        spin_eq(flr + (F_R5 - L_FL), 9);
        float v = *lptr<float>(a_rp + 512), v8 = *lptr<float>(LB + L_RP + 512 + 256 + (l & 7) * 4);
        v += dpp<0x128>(v);
        v = x32_sum(x16_sum(v)) + v8;
        float ov = hv + rbf(v);
        unsigned op2 = pack2(ov, dpp<0x101>(ov));
        if (l < 8 && !(l & 1)) *(unsigned*)(out + rd * 8 + l) = op2;
        if (rd == 0 && l == 0) __hip_atomic_store((gu64*)(ws + WS_EPOCH), epoch, __ATOMIC_RELAXED, __HIP_MEMORY_SCOPE_AGENT);
      }
    }
  } else {
    const unsigned tag32 = (unsigned)epoch;
    unsigned o_hb = WS_HB + (xcd * 2 + (f & 1)) * HB_STRIDE + (32 * w + 16 * ((n16 >> 1) & 1) + 4 * kg + 2 * (n16 & 1)) * 8;
    unsigned a_w1 = LB + L_W + w * 4096 + (8 * kg + q4) * 64 + (((c4 >> 1) ^ (2 * (kg & 1))) * 16) + (c4 & 1) * 8;
    unsigned a_ps = LB + L_PART + n16 * 48 + w * 4;
    unsigned a_pt = LB + L_PART + (w < 8 ? 8 * (l & 3) + w : 32 + 8 * (l & 3)) * 48 + 40;
    unsigned a_pr = LB + L_PART + ((n16 < 8 ? 0 : 32) + 8 * kg + (n16 & 7)) * 48;
    unsigned o_h5 = WS_H5 + (((18 * w + (l >> 3)) * 144 + f) * 8 + (l & 7)) * 8, o_h5b = o_h5 + 73728, o_h5c = o_h5 + 2 * 73728;
    PIN(o_hb); PIN(a_w1); PIN(a_ps); PIN(a_pt); PIN(a_pr); PIN(o_h5); PIN(o_h5b); PIN(o_h5c);
    spin1(LB + F_W1);
    bf16x8 b1[2][2], b2[9];
#pragma unroll
    for (int ks = 0; ks < 2; ++ks)
#pragma unroll
      for (int nb = 0; nb < 2; ++nb) b1[ks][nb] = cat(trd((a_w1 ^ (nb * 32)) + ks * 2048), trd((a_w1 ^ (nb * 32)) + ks * 2048 + 256));
    const u32x4 on = {0x3F803F80u, 0x3F803F80u, 0x3F803F80u, 0x3F803F80u};
    const bf16x8 one = __builtin_bit_cast(bf16x8, on);
    const f32x4 z4 = {0.f, 0.f, 0.f, 0.f};
    {
      f32x4 c0 = mfma32(one, b1[0][0], z4), c1 = mfma32(one, b1[0][1], z4);
      c0 = mfma32(one, b1[1][0], c0); c1 = mfma32(one, b1[1][1], c1);
      if (l < 16) { *lptr<float>(a_ps + 32 * 48) = c0[0]; *lptr<float>(a_ps + 48 * 48) = c1[0]; }
    }
    if (w < 4) {
      spin1(LB + F_W);
#pragma unroll
      for (int i = 0; i < 9; ++i) {
        const unsigned a = ((LB + L_W2 + trb) ^ (((w + i) & 3) * 32)) + ((9 * w + i) >> 2) * 4096;
        b2[i] = cat(trd(a), trd(a + 512));
      }
    }
    u32x4 hg;
    _Pragma("unroll 1") for (unsigned it = 0; it < SPIN_MAX; ++it) {
      ld16_sc1_wait(hg, o_hb, rs_ws);
      if (allok((hg.y ^ tag32) | (hg.w ^ tag32))) break;
    }
    {
      u32x4 A0 = {dppu<0x150>(hg.x), dppu<0x150>(hg.z), dppu<0x151>(hg.x), dppu<0x151>(hg.z)};
      u32x4 A1 = {dppu<0x152>(hg.x), dppu<0x152>(hg.z), dppu<0x153>(hg.x), dppu<0x153>(hg.z)};
      bf16x8 a0 = __builtin_bit_cast(bf16x8, A0), a1 = __builtin_bit_cast(bf16x8, A1);
      f32x4 d0 = mfma32(a0, b1[0][0], z4), d1 = mfma32(a0, b1[0][1], z4), e1 = mfma32(a0, one, z4), e2 = mfma32(a0, a0, z4);
      d0 = mfma32(a1, b1[1][0], d0); d1 = mfma32(a1, b1[1][1], d1); e1 = mfma32(a1, one, e1); e2 = mfma32(a1, a1, e2);
      if (l < 16) { *lptr<float>(a_ps) = d0[0]; *lptr<float>(a_ps + 16 * 48) = d1[0]; }
      if (l < 4) *lptr<f32x2>(a_pt) = (f32x2){e1[0], e2[0]};
    }
    BAR();
    if (w < 4) {
      f32x4 u0 = *lptr<f32x4>(a_pr), u1 = *lptr<f32x4>(a_pr + 16), u2 = *lptr<f32x4>(a_pr + 32);
      float T = ((u0[0] + u0[1]) + (u0[2] + u0[3])) + ((u1[0] + u1[1]) + (u1[2] + u1[3])) + u2[0];
      float s1 = n16 < 9 ? u2[2] : 0.f, s2 = n16 < 9 ? u2[3] : 0.f;
      s1 = row16_sum(s1); s2 = row16_sum(s2);
      float mean = s1 * (1.f / 576.f), var = fmaxf(s2 * (1.f / 576.f) - mean * mean, 0.f);
      float ri = __builtin_amdgcn_rsqf(var + 1e-5f);
      float C = dpp<0x128>(T);
      float a = rbf((T - mean * C) * ri);
      float act = a * __builtin_amdgcn_rcpf(1.f + fexp(-a));
      unsigned pk = pack2(act, dpp<0x101>(act));
      u32x4 A = {dppu<0x150>(pk), dppu<0x152>(pk), dppu<0x154>(pk), dppu<0x156>(pk)};
      bf16x8 af = __builtin_bit_cast(bf16x8, A);
      f32x4 d[9];
#pragma unroll
      for (int i = 0; i < 9; ++i) d[i] = mfma32(af, b2[i], z4);
      put(ws, o_h5, sel4(kg, d[0][0], d[1][0], d[2][0], d[3][0]), tag);
      put(ws, o_h5b, sel4(kg, d[4][0], d[5][0], d[6][0], d[7][0]), tag);
      if (l < 16) put(ws, o_h5c, d[8][0], tag);
    }
  }
}

extern "C" void kernel_launch(void* const* d_in, const int* in_sizes, int n_in, void* d_out, int out_size, void* d_ws, size_t ws_size,
                              hipStream_t stream) {
  fused_decode_kernel<<<256, NT, 0, stream>>>((const u16*)d_in[0], (const u16*)d_in[1], (const u16*)d_in[2], (const u16*)d_in[3],
                                              (const u16*)d_in[4], (const u16*)d_in[6], (const u16*)d_in[7], (u16*)d_out,
                                              (unsigned char*)d_ws);
}
```
